```python
import torch

def _softmax(x, axis=-1):
    return torch.softmax(x, dim=axis)
def _gelu(x):
    return torch.nn.functional.gelu(x, approximate="tanh")
def _silu(x):
    return torch.nn.functional.silu(x)
def _sigmoid(x):
    return torch.sigmoid(x)
def _layernorm(x, axis=-1, eps=1e-5):
    m = x.mean(dim=axis, keepdim=True)
    v = x.var(dim=axis, keepdim=True, unbiased=False)
    return (x - m) / torch.sqrt(v + eps)
def _scatter_add(tgt, idx, src, axis=0):
    return tgt.clone().index_add_(axis, idx, src)
def _segment_sum(src, seg, n):
    out = torch.zeros((n,) + src.shape[1:], dtype=src.dtype)
    return out.index_add_(0, seg, src)
def _pad(x, pw):
    flat = tuple(v for pair in reversed(pw) for v in pair)
    return torch.nn.functional.pad(x, flat)

kernel_name = 'concat_heads__mha_block_cached__mha_block_cached__near_ridge'

def setup_inputs():

    _w = (lambda a: (lambda t: t.to(torch.bfloat16) if t.is_floating_point() else t)(torch.as_tensor(a)))
    def _A_setup():
        B, Sq, D = 8, 144, 640
        H, Sk, Dh = 32, 512, 192
        return {
            "x": _w((lambda *s: torch.randn(*s, dtype=torch.bfloat16) * 0.8)(B, Sq, D)),
            "k_cache": _w((lambda *s: torch.randn(*s, dtype=torch.bfloat16) * 0.8)(B, H, Sk, Dh)),
            "v_cache": _w((lambda *s: torch.randn(*s, dtype=torch.bfloat16) * 0.05)(B, H, Sk, Dh)),
            "wq": _w((lambda *s: torch.randn(*s, dtype=torch.bfloat16) * 0.8)(D, H * Dh) * (D ** -0.5)),
            "wo": _w((lambda *s: torch.randn(*s, dtype=torch.bfloat16) * 0.05)(H * Dh, D) * ((H * Dh) ** -0.5)),
        }
    _A_in = _A_setup()

    def _B_setup():
        B, Sq, D = 8, 144, 640
        H, Sk, Dh = 32, 512, 192
        return {
            "x": _w((lambda *s: torch.randn(*s, dtype=torch.bfloat16) * 0.8)(B, Sq, D)),
            "k_cache": _w((lambda *s: torch.randn(*s, dtype=torch.bfloat16) * 0.8)(B, H, Sk, Dh)),
            "v_cache": _w((lambda *s: torch.randn(*s, dtype=torch.bfloat16) * 0.05)(B, H, Sk, Dh)),
            "wq": _w((lambda *s: torch.randn(*s, dtype=torch.bfloat16) * 0.8)(D, H * Dh) * (D ** -0.5)),
            "wo": _w((lambda *s: torch.randn(*s, dtype=torch.bfloat16) * 0.05)(H * Dh, D) * ((H * Dh) ** -0.5)),
        }
    _B_in = _B_setup()

    return {
        "A_x": _A_in["x"],
        "A_k_cache": _A_in["k_cache"],
        "A_v_cache": _A_in["v_cache"],
        "A_wq": _A_in["wq"],
        "A_wo": _A_in["wo"],
        "B_x": _B_in["x"],
        "B_k_cache": _B_in["k_cache"],
        "B_v_cache": _B_in["v_cache"],
        "B_wq": _B_in["wq"],
        "B_wo": _B_in["wo"],
    }

def reference(A_x, A_k_cache, A_v_cache, A_wq, A_wo, B_x, B_k_cache, B_v_cache, B_wq, B_wo):
    def _A_ref(x, k_cache, v_cache, wq, wo):
        B, Sq, D = x.shape
        H, Dh = 32, 192
        q = (lambda x, *d: x.permute(*d))((x @ wq).reshape(B, Sq, H, Dh), 0, 2, 1, 3)
        scores = torch.einsum("bhqd,bhkd->bhqk", q, k_cache) * (Dh ** -0.5)
        p = _softmax(scores, axis=-1)
        o = torch.einsum("bhqk,bhkd->bhqd", p, v_cache)
        return (lambda x, *d: x.permute(*d))(o, 0, 2, 1, 3).reshape(B, Sq, H * Dh) @ wo

    def _B_ref(x, k_cache, v_cache, wq, wo):
        B, Sq, D = x.shape
        H, Dh = 32, 192
        q = (lambda x, *d: x.permute(*d))((x @ wq).reshape(B, Sq, H, Dh), 0, 2, 1, 3)
        scores = torch.einsum("bhqd,bhkd->bhqk", q, k_cache) * (Dh ** -0.5)
        p = _softmax(scores, axis=-1)
        o = torch.einsum("bhqk,bhkd->bhqd", p, v_cache)
        return (lambda x, *d: x.permute(*d))(o, 0, 2, 1, 3).reshape(B, Sq, H * Dh) @ wo

    a = _A_ref(A_x, A_k_cache, A_v_cache, A_wq, A_wo)
    b = _B_ref(B_x, B_k_cache, B_v_cache, B_wq, B_wo)
    return torch.concatenate([a, b], axis=-1)
```

```cpp
#pragma clang diagnostic ignored "-Wint-to-pointer-cast"
#include <hip/hip_runtime.h>
#include <hip/hip_bf16.h>
#include <stdint.h>

typedef __attribute__((ext_vector_type(8))) short bf16x8;
typedef __attribute__((ext_vector_type(4))) short bf16x4;
typedef __attribute__((ext_vector_type(4))) float f32x4;
typedef __attribute__((ext_vector_type(2))) float f32x2;
typedef __attribute__((ext_vector_type(2))) __bf16 bf2;
typedef __attribute__((ext_vector_type(2))) unsigned u32x2;
typedef __attribute__((ext_vector_type(4))) unsigned u32x4;
#define LDSP(T) __attribute__((address_space(3))) T*

constexpr int SQ = 144, DM = 640, NH = 32, SK = 512, DH = 192, HD = 6144;
constexpr int XBUF = 18432, SLOT = 24576, RING = 55296, STATM = 153600, STATS = 158208, LDS1 = 162816;

template <int I> struct IC { static constexpr int v = I; };
template <int B, int E, class F> __device__ __forceinline__ void sfor(F&& f) {
  if constexpr (B < E) { f(IC<B>{}); sfor<B + 1, E>(f); }
}
template <int N> __device__ __forceinline__ void wait_vm() { asm volatile("s_waitcnt vmcnt(%0)" ::"i"(N) : "memory"); }
__device__ __forceinline__ void bar() {
  __builtin_amdgcn_sched_barrier(0);
  asm volatile("s_waitcnt lgkmcnt(0)\n\ts_barrier" ::: "memory");
  __builtin_amdgcn_sched_barrier(0);
}
__device__ __forceinline__ void dma16(unsigned voff, const void* base, unsigned lds) {
  asm volatile("s_mov_b32 m0, %1\n\ts_nop 0\n\tglobal_load_lds_dwordx4 %0, %2" ::"v"(voff), "s"(lds), "s"(base) : "memory");
}
__device__ __forceinline__ void dma16nt(unsigned voff, const void* base, unsigned lds) {
  asm volatile("s_mov_b32 m0, %1\n\ts_nop 0\n\tglobal_load_lds_dwordx4 %0, %2 nt" ::"v"(voff), "s"(lds), "s"(base) : "memory");
}
template <int OFF> __device__ __forceinline__ void gload_nt(bf16x8& d, unsigned voff, const void* base) {
  asm volatile("global_load_dwordx4 %0, %1, %2 offset:%3 nt" : "=v"(d) : "v"(voff), "s"(base), "i"(OFF) : "memory");
}
__device__ __forceinline__ void pin(bf16x8& d) { asm volatile("" : "+v"(d)); }
__device__ __forceinline__ void pinf(float& d) { asm volatile("" : "+v"(d)); }
__device__ __forceinline__ unsigned opq(unsigned d) { asm volatile("" : "+v"(d)); return d; }
typedef LDSP(char) lp;
__device__ __forceinline__ lp mkp(unsigned a) { return (lp)opq(a); }
__device__ __forceinline__ bf16x8 lds128(lp a) { return *(LDSP(bf16x8))a; }
__device__ __forceinline__ bf16x8 ldstr(lp a, int d) {
  bf16x4 lo = __builtin_amdgcn_ds_read_tr16_b64_v4i16((LDSP(bf16x4))a);
  bf16x4 hi = __builtin_amdgcn_ds_read_tr16_b64_v4i16((LDSP(bf16x4))(a + d));
  return __builtin_shufflevector(lo, hi, 0, 1, 2, 3, 4, 5, 6, 7);
}
__device__ __forceinline__ unsigned pk2(float a, float b) {
  f32x2 v = {a, b};
  return __builtin_bit_cast(unsigned, __builtin_convertvector(v, bf2));
}
__device__ __forceinline__ f32x4 mma(bf16x8 a, bf16x8 b, f32x4 c) {
  typedef __attribute__((ext_vector_type(8))) __bf16 b8;
  return __builtin_amdgcn_mfma_f32_16x16x32_bf16(__builtin_bit_cast(b8, a), __builtin_bit_cast(b8, b), c, 0, 0, 0);
}
__device__ __forceinline__ float xg_max(float v) {
  unsigned u = __float_as_uint(v);
  auto r = __builtin_amdgcn_permlane16_swap(u, u, false, false);
  v = fmaxf(__uint_as_float(r[0]), __uint_as_float(r[1]));
  u = __float_as_uint(v);
  auto s = __builtin_amdgcn_permlane32_swap(u, u, false, false);
  return fmaxf(__uint_as_float(s[0]), __uint_as_float(s[1]));
}
__device__ __forceinline__ float xg_sum(float v) {
  unsigned u = __float_as_uint(v);
  auto r = __builtin_amdgcn_permlane16_swap(u, u, false, false);
  v = __uint_as_float(r[0]) + __uint_as_float(r[1]);
  u = __float_as_uint(v);
  auto s = __builtin_amdgcn_permlane32_swap(u, u, false, false);
  return __uint_as_float(s[0]) + __uint_as_float(s[1]);
}

struct P1 {
  const char* x[2]; const char* k[2]; const char* v[2]; const char* wq[2];
  char* ofr;
};

template <int NQ, int QT0, int HALF>
__device__ __forceinline__ void k1_body(const P1& p, int w, int lane, int h, int b) {
  const int g = lane >> 4, c = lane & 15, dq = w & 3;
  unsigned offW[3], offV[3], offX[3];
  sfor<0, 3>([&](auto I) {
    constexpr int pp = I.v;
    int o = 1024 * pp + 16 * lane, rr = o / 384, ch = (o % 384) >> 4;
    int hh = ((rr >> 1) & 1) | ((w & 1) << 1);
    int sc = (((ch >> 1) ^ hh) << 1) | (ch & 1);
    offV[pp] = (8 * w + rr) * 384 + sc * 16;
    offW[pp] = (8 * w + rr) * (HD * 2) + sc * 16;
    int row = 18 * w + 8 * pp + (lane >> 3);
    offX[pp] = row * (DM * 2) + (((lane & 7) ^ ((row >> 1) & 7)) << 4);
  });
  const unsigned ldsW = RING + w * 3072, ldsX = w * 2304;
  const unsigned offK = (16 * w + c) * 384 + g * 16;
  lp adT[3];
  int ut[3];
  sfor<0, 3>([&](auto I) {
    constexpr int t = I.v;
    ut[t] = 3 * dq + ((dq & 1) ? (t + 1) % 3 : t);
    int q4 = c >> 2, hh = ((q4 >> 1) & 1) | ((g & 1) << 1);
    adT[t] = mkp(RING + (8 * g + q4) * 384 + ((ut[t] ^ hh) << 5) + ((c & 3) << 3));
  });
  lp adX[2];
  adX[0] = mkp((QT0 * 16 + c) * 128 + (((0 + g) ^ ((c >> 1) & 7)) << 4));
  adX[1] = mkp((QT0 * 16 + c) * 128 + (((4 + g) ^ ((c >> 1) & 7)) << 4));
  const lp adL = mkp(lane * 16);
  const unsigned adH = ((g >> 1) * 16 + c) * 16 + (g & 1) * 8;
  const float CS = 0.07216878364870322f * 1.4426950408889634f;

  auto dW = [&](const char* wb, int i, auto I) { dma16(offW[I.v], wb + (size_t)i * 64 * HD * 2, ldsW + (i & 3) * SLOT + I.v * 1024); };
  auto dX = [&](const char* xb, int i, auto I) {
    unsigned l = ldsX + ((i + 2) % 3) * XBUF + I.v * 1024;
    if constexpr (I.v < 2) dma16(offX[I.v], xb + i * 128, l);
    else if (lane < 16) dma16(offX[2], xb + i * 128, l);
  };
  auto dV = [&](const char* vb, int i, auto I) { dma16nt(offV[I.v], vb + i * SLOT, ldsW + (i & 3) * SLOT + I.v * 1024); };
  auto issueW = [&](const char* wb, int i) { sfor<0, 3>([&](auto I) { dW(wb, i, I); }); };
  auto issueX = [&](const char* xb, int i) { sfor<0, 3>([&](auto I) { dX(xb, i, I); }); };
  constexpr int NST = 2 * NQ, NM = 3 * NQ, NR = 3 + NQ;

  const size_t xo = (size_t)b * SQ * DM * 2, wo = (size_t)h * DH * 2, kvo = (size_t)(b * NH + h) * SK * DH * 2;
  {
    const char* wb = p.wq[0] + wo; const char* xb = p.x[0] + xo;
    issueW(wb, 0); issueW(wb, 1); issueX(xb, 0); issueW(wb, 2); issueX(xb, 1); issueW(wb, 3); issueX(xb, 2);
    wait_vm<12>(); bar();
  }
#pragma unroll 1
  for (int u = 0; u < 2; ++u) {
    const char* wb = (u ? p.wq[1] : p.wq[0]) + wo;
    const char* xb = (u ? p.x[1] : p.x[0]) + xo;
    const char* kb = (u ? p.k[1] : p.k[0]) + kvo;
    const char* vb = (u ? p.v[1] : p.v[0]) + kvo;
    bf16x8 kf[4][6];
    bf16x8 fa[2][3], fb[2][NQ];
    f32x4 acc[3][NQ];
    auto rdT = [&](auto S, auto KS, auto T) {
      constexpr int i = S.v, ks = KS.v;
      if constexpr ((i & 3) == 3) fa[ks][T.v] = ldstr(mkp((unsigned)(size_t)adT[T.v] + 3 * SLOT) + ks * 12288, 1536);
      else fa[ks][T.v] = ldstr(adT[T.v] + (i & 3) * SLOT + ks * 12288, 1536);
    };
    auto seg = [&](auto KS, auto NI, auto&& item) {
      sfor<0, NM>([&](auto M) {
        constexpr int q = M.v / 3, t = M.v % 3;
        acc[t][q] = mma(fa[KS.v][t], fb[KS.v][q], acc[t][q]);
        __builtin_amdgcn_sched_barrier(0);
        constexpr int per = (NI.v + NM - 1) / NM, lo = M.v * per < NI.v ? M.v * per : NI.v, hi = lo + per < NI.v ? lo + per : NI.v;
        sfor<lo, hi>([&](auto K) { item(K); __builtin_amdgcn_sched_barrier(0); });
      });
    };
    {
      sfor<0, 3>([&](auto T) { sfor<0, NQ>([&](auto Q) { acc[T.v][Q.v] = f32x4{0, 0, 0, 0}; }); });
      auto rd1 = [&](auto S, auto KS, auto R) {
        if constexpr (R.v == 0) fb[KS.v][0] = lds128(adX[KS.v] + ((S.v + 2) % 3) * XBUF);
        else if constexpr (R.v <= 3) rdT(S, KS, IC<R.v - 1>{});
        else fb[KS.v][R.v - 3] = lds128(adX[KS.v] + ((S.v + 2) % 3) * XBUF + (R.v - 3) * 2048);
      };
      auto vm = [&](auto J, auto N) {
        constexpr int j = J.v, n = N.v;
        constexpr int nx = j <= 6 ? 3 : 0, nw = j <= 5 ? 3 : 0;
        if constexpr (j <= 7) {
          if constexpr (n < nx) dX(xb, j + 3, IC<n>{});
          else if constexpr (n < nx + nw) dW(wb, j + 4, IC<n - nx>{});
          else { constexpr int m = 3 * j + n - nx - nw, pr = m / 12, s = (m % 12) / 2, jt = 2 * pr + (m & 1);
                 gload_nt<s * 64>(kf[jt][s], offK, kb + jt * 49152); }
        } else if constexpr (j == 8) dV(vb, 0, N);
        else dV(vb, 1 + n / 3, IC<n % 3>{});
      };
      sfor<0, NR>([&](auto R) { rd1(IC<0>{}, IC<0>{}, R); });
      __builtin_amdgcn_sched_barrier(0);
      sfor<0, 10>([&](auto S) {
        constexpr int i = S.v;
        constexpr int pv = i == 0 ? 0 : (i - 1 <= 5 ? 9 : i - 1 == 6 ? 6 : 3), p1 = NM - NR < pv ? NM - NR : pv;
        seg(IC<0>{}, IC<NR + pv - p1>{}, [&](auto K) {
          if constexpr (K.v < NR) rd1(S, IC<1>{}, K); else vm(IC<i - 1>{}, IC<p1 + K.v - NR>{});
        });
        if constexpr (i == 0) { if (u == 0) wait_vm<6>(); else wait_vm<6 + NST>(); }
        else if constexpr (i == 1) { if (u == 0) wait_vm<9>(); else wait_vm<9 + NST>(); }
        else if constexpr (i <= 6) wait_vm<15>();
        else if constexpr (i == 7) wait_vm<12>();
        else if constexpr (i == 8) wait_vm<6>();
        bar();
        constexpr int cv = i <= 5 ? 9 : i == 6 ? 6 : i == 9 ? 9 : 3;
        if constexpr (i < 9) {
          constexpr int c1 = NM - NR < cv ? NM - NR : cv;
          seg(IC<1>{}, IC<NR + c1>{}, [&](auto K) {
            if constexpr (K.v < NR) rd1(IC<i + 1>{}, IC<0>{}, K); else vm(S, IC<K.v - NR>{});
          });
        } else seg(IC<1>{}, IC<cv>{}, [&](auto K) { vm(S, K); });
      });
      sfor<0, 3>([&](auto T) {
        lp a = mkp(adH + (ut[T.v] >> 1) * 1024 + (ut[T.v] & 1) * 512) + QT0 * 6144;
        sfor<0, NQ>([&](auto Q) {
          f32x4 v = acc[T.v][Q.v];
          *(LDSP(u32x2))(a + Q.v * 6144) = u32x2{pk2(v[0], v[1]), pk2(v[2], v[3])};
        });
      });
      bar();
    }
    f32x4 sa[4][9];
    float mx[9];
    const lp adS = mkp((opq(lane) & 15) * 32 + STATM), adSw = mkp((opq(lane) & 15) * 32 + STATM + w * 4), adSg = mkp((opq(lane) & 15) * 32 + STATM + g * 8);
    {
      constexpr int PF = 3;
      bf16x8 qf[PF + 1];
      auto rowmax = [&](auto PR, auto Q) {
        f32x4 a = sa[2 * PR.v][Q.v], bb = sa[2 * PR.v + 1][Q.v];
        float m = fmaxf(fmaxf(fmaxf(a[0], a[1]), fmaxf(a[2], a[3])), fmaxf(fmaxf(bb[0], bb[1]), fmaxf(bb[2], bb[3])));
        if constexpr (PR.v == 0) mx[Q.v] = m;
        else { m = xg_max(fmaxf(mx[Q.v], m)); if (g == 0) *(LDSP(float))(adSw + Q.v * 512) = m; }
      };
      sfor<0, PF>([&](auto N) { qf[N.v] = lds128(adL + N.v * 1024); });
      sfor<0, 6>([&](auto S) { pin(kf[0][S.v]); pin(kf[1][S.v]); });
      __builtin_amdgcn_sched_barrier(0);
      sfor<0, 108>([&](auto N) {
        constexpr int n = N.v, pr = n / 54, q = (n % 54) / 6, s = n % 6;
        if constexpr (n == 54) {
          wait_vm<12>();
          sfor<0, 6>([&](auto S) { pin(kf[2][S.v]); pin(kf[3][S.v]); });
          __builtin_amdgcn_sched_barrier(0);
        }
        if constexpr (s == 0) { sa[2 * pr][q] = f32x4{0, 0, 0, 0}; sa[2 * pr + 1][q] = f32x4{0, 0, 0, 0}; }
        sa[2 * pr][q] = mma(kf[2 * pr][s], qf[n % (PF + 1)], sa[2 * pr][q]);
        __builtin_amdgcn_sched_barrier(0);
        if constexpr (n + PF < 108) qf[(n + PF) % (PF + 1)] = lds128(adL + ((n + PF) % 54) * 1024);
        __builtin_amdgcn_sched_barrier(0);
        sa[2 * pr + 1][q] = mma(kf[2 * pr + 1][s], qf[n % (PF + 1)], sa[2 * pr + 1][q]);
        __builtin_amdgcn_sched_barrier(0);
        if constexpr (s == 2 && n >= 6) { constexpr int pn = n - 6; rowmax(IC<pn / 54>{}, IC<(pn % 54) / 6>{}); __builtin_amdgcn_sched_barrier(0); }
      });
      rowmax(IC<1>{}, IC<8>{});
    }
    bar();
    float sm[9];
    sfor<0, 9>([&](auto Q) {
      f32x2 v = *(LDSP(f32x2))(adSg + Q.v * 512);
      mx[Q.v] = -CS * xg_max(fmaxf(v[0], v[1]));
      sm[Q.v] = 0.f;
    });
    const lp adP = mkp(adH + (((w & 3) >> 1) * 9) * 1024 + (w & 1) * 512);
    auto exph = [&](auto J, auto K) {
      constexpr int j = J.v, pb = ((2 * j + HALF) % 3) * XBUF, q = K.v / 2;
      if constexpr ((K.v & 1) == 0) {
        sfor<0, 4>([&](auto E) { sa[j][q][E.v] = __builtin_amdgcn_exp2f(fmaf(sa[j][q][E.v], CS, mx[q])); });
      } else {
        f32x4 e = sa[j][q];
        sm[q] += (e[0] + e[1]) + (e[2] + e[3]);
        if constexpr (j == 3) { sm[q] = xg_sum(sm[q]); if (g == 0) *(LDSP(float))(adSw + (STATS - STATM) + q * 512) = sm[q]; }
        else pinf(sm[q]);
        *(LDSP(u32x2))(adP + pb + q * 1024) = u32x2{pk2(e[0], e[1]), pk2(e[2], e[3])};
      }
    };
    sfor<0, 18>([&](auto K) { exph(IC<0>{}, K); });
    __builtin_amdgcn_sched_barrier(0);
    wait_vm<9>();
    bar();
    {
      const char* wn = p.wq[1] + wo; const char* xn = p.x[1] + xo;
      sfor<0, 3>([&](auto T) { sfor<0, NQ>([&](auto Q) { acc[T.v][Q.v] = f32x4{0, 0, 0, 0}; }); });
      auto rd1 = [&](auto S, auto KS, auto R) {
        if constexpr (R.v == 0) fb[KS.v][0] = lds128(adL + (S.v % 3) * XBUF + (KS.v * 9 + QT0) * 1024);
        else if constexpr (R.v <= 3) rdT(S, KS, IC<R.v - 1>{});
        else fb[KS.v][R.v - 3] = lds128(adL + (S.v % 3) * XBUF + (KS.v * 9 + QT0 + R.v - 3) * 1024);
      };
      auto vm = [&](auto J, auto N) {
        constexpr int j = J.v, n = N.v;
        if constexpr (j <= 3) dV(vb, j + 4, N);
        else if (u == 0) { if constexpr (n < 3) dW(wn, j - 4, N); else dX(xn, j - 5, IC<n - 3>{}); }
      };
      sfor<0, NR>([&](auto R) { rd1(IC<0>{}, IC<0>{}, R); });
      __builtin_amdgcn_sched_barrier(0);
      if constexpr (HALF == 0) { sfor<0, 8>([&](auto K) { exph(IC<1>{}, K); }); __builtin_amdgcn_sched_barrier(0); }
      sfor<0, 8>([&](auto S) {
        constexpr int i = S.v;
        constexpr bool e0 = (i <= 5) && (((i + 1) & 1) != HALF);
        constexpr int j0 = (i + 2 - HALF) / 2;
        seg(IC<0>{}, IC<NR + (e0 ? 10 : 0)>{}, [&](auto K) {
          if constexpr (!e0) rd1(S, IC<1>{}, K);
          else if constexpr (K.v < 2 * NR) { if constexpr (K.v & 1) exph(IC<j0>{}, IC<8 + K.v / 2>{}); else rd1(S, IC<1>{}, IC<K.v / 2>{}); }
          else exph(IC<j0>{}, IC<8 + K.v - NR>{});
        });
        if constexpr (i <= 4) wait_vm<6>();
        else if constexpr (i == 5) { if (u == 0) wait_vm<6>(); else wait_vm<3>(); }
        else if constexpr (i == 6) { if (u == 0) wait_vm<9>(); else wait_vm<0>(); }
        else { if (u == 0) wait_vm<6>(); }
        bar();
        constexpr bool e1 = (i <= 4) && ((i & 1) != HALF);
        constexpr int j1 = (i + 3 - HALF) / 2, cv = i <= 4 ? 3 : 6, nr = i < 7 ? NR : 0;
        seg(IC<1>{}, IC<nr + cv + (e1 ? 8 : 0)>{}, [&](auto K) {
          if constexpr (!e1) { if constexpr (K.v < nr) rd1(IC<i + 1>{}, IC<0>{}, K); else vm(S, IC<K.v - nr>{}); }
          else if constexpr (K.v < 2 * NR) { if constexpr (K.v & 1) exph(IC<j1>{}, IC<K.v / 2>{}); else rd1(IC<i + 1>{}, IC<0>{}, IC<K.v / 2>{}); }
          else if constexpr (K.v < NR + 8) exph(IC<j1>{}, IC<K.v - NR>{});
          else vm(S, IC<K.v - NR - 8>{});
        });
      });
      char* ob = p.ofr + ((size_t)((u * 8 + b) * 192 + h * 6) * 9 + QT0) * 1024 + opq(lane * 16);
      const int sf = ut[0] >> 1, sh = ut[2] >> 1, hp = ut[2] & 1;
      sfor<0, NQ>([&](auto Q) {
        f32x4 s0 = *(LDSP(f32x4))(adS + (STATS - STATM) + (QT0 + Q.v) * 512), s1 = *(LDSP(f32x4))(adS + (STATS - STATM) + (QT0 + Q.v) * 512 + 16);
        float r = __builtin_amdgcn_rcpf(((s0[0] + s0[1]) + (s0[2] + s0[3])) + ((s1[0] + s1[1]) + (s1[2] + s1[3])));
        f32x4 a0 = acc[0][Q.v] * r, a1 = acc[1][Q.v] * r, a2 = acc[2][Q.v] * r;
        *(u32x4*)(ob + (sf * 9 + Q.v) * 1024) = u32x4{pk2(a0[0], a0[1]), pk2(a0[2], a0[3]), pk2(a1[0], a1[1]), pk2(a1[2], a1[3])};
        *(u32x2*)(ob + (sh * 9 + Q.v) * 1024 + hp * 8) = u32x2{pk2(a2[0], a2[1]), pk2(a2[2], a2[3])};
      });
      __builtin_amdgcn_sched_barrier(0);
    }
  }
}

__global__ __launch_bounds__(512, 2) void k1_attn(P1 p) {
  extern __shared__ __attribute__((aligned(16))) char smem[];
  const int w = __builtin_amdgcn_readfirstlane(threadIdx.x >> 6), lane = threadIdx.x & 63;
  const int xcd = blockIdx.x & 7, idx = blockIdx.x >> 3;
  const int h = 4 * xcd + (idx & 3), b = idx >> 2;
  if (w < 4) k1_body<5, 0, 0>(p, w, lane, h, b);
  else k1_body<4, 5, 1>(p, w, lane, h, b);
}

__global__ void k2_naive(const __hip_bfloat16* ofr, const __hip_bfloat16* woA, const __hip_bfloat16* woB, __hip_bfloat16* out) {
  int n = blockIdx.x * 128 + threadIdx.x;
  int row = blockIdx.y;
  int br = n / 640, nn = n % 640, b = row / 144, q = row % 144, qt = q >> 4, c = q & 15;
  const __hip_bfloat16* wo = br ? woB : woA;
  float acc = 0.f;
  for (int ks = 0; ks < 192; ++ks)
    for (int g = 0; g < 4; ++g)
      for (int j = 0; j < 8; ++j) {
        int hd = ks * 32 + 16 * (j >> 2) + 4 * g + (j & 3);
        float o = __bfloat162float(ofr[((size_t)(((br * 8 + b) * 192 + ks) * 9 + qt) * 64 + g * 16 + c) * 8 + j]);
        acc += o * __bfloat162float(wo[(size_t)hd * 640 + nn]);
      }
  out[(size_t)row * 1280 + n] = __float2bfloat16(acc);
}

extern "C" void kernel_launch(void* const* d_in, const int* in_sizes, int n_in, void* d_out, int out_size, void* d_ws,
                              size_t ws_size, hipStream_t stream) {
  P1 p;
  p.x[0] = (const char*)d_in[0]; p.k[0] = (const char*)d_in[1]; p.v[0] = (const char*)d_in[2]; p.wq[0] = (const char*)d_in[3];
  p.x[1] = (const char*)d_in[5]; p.k[1] = (const char*)d_in[6]; p.v[1] = (const char*)d_in[7]; p.wq[1] = (const char*)d_in[8];
  p.ofr = (char*)d_ws;
  (void)hipFuncSetAttribute((const void*)k1_attn, hipFuncAttributeMaxDynamicSharedMemorySize, LDS1);
  k1_attn<<<256, 512, LDS1, stream>>>(p);
  k2_naive<<<dim3(10, 1152), 128, 0, stream>>>((const __hip_bfloat16*)d_ws, (const __hip_bfloat16*)d_in[4], (const __hip_bfloat16*)d_in[9], (__hip_bfloat16*)d_out);
}
```

```cpp
#pragma clang diagnostic ignored "-Wint-to-pointer-cast"
#include <hip/hip_runtime.h>
#include <hip/hip_bf16.h>
#include <stdint.h>

typedef __attribute__((ext_vector_type(8))) short bf16x8;
typedef __attribute__((ext_vector_type(4))) short bf16x4;
typedef __attribute__((ext_vector_type(4))) float f32x4;
typedef __attribute__((ext_vector_type(2))) float f32x2;
typedef __attribute__((ext_vector_type(2))) __bf16 bf2;
typedef __attribute__((ext_vector_type(2))) unsigned u32x2;
typedef __attribute__((ext_vector_type(4))) unsigned u32x4;
#define LDSP(T) __attribute__((address_space(3))) T*

constexpr int SQ = 144, DM = 640, NH = 32, SK = 512, DH = 192, HD = 6144;
constexpr int XBUF = 18432, SLOT = 24576, RING = 55296, STATM = 153600, STATS = 158208, LDS1 = 162816;

template <int I> struct IC { static constexpr int v = I; };
template <int B, int E, class F> __device__ __forceinline__ void sfor(F&& f) {
  if constexpr (B < E) { f(IC<B>{}); sfor<B + 1, E>(f); }
}
template <int N> __device__ __forceinline__ void wait_vm() { asm volatile("s_waitcnt vmcnt(%0)" ::"i"(N) : "memory"); }
__device__ __forceinline__ void bar() {
  __builtin_amdgcn_sched_barrier(0);
  asm volatile("s_waitcnt lgkmcnt(0)\n\ts_barrier" ::: "memory");
  __builtin_amdgcn_sched_barrier(0);
}
__device__ __forceinline__ void dma16(unsigned voff, const void* base, unsigned lds) {
  asm volatile("s_mov_b32 m0, %1\n\ts_nop 0\n\tglobal_load_lds_dwordx4 %0, %2" ::"v"(voff), "s"(lds), "s"(base) : "memory");
}
__device__ __forceinline__ void dma16nt(unsigned voff, const void* base, unsigned lds) {
  asm volatile("s_mov_b32 m0, %1\n\ts_nop 0\n\tglobal_load_lds_dwordx4 %0, %2 nt" ::"v"(voff), "s"(lds), "s"(base) : "memory");
}
template <int OFF> __device__ __forceinline__ void gload_nt(bf16x8& d, unsigned voff, const void* base) {
  asm volatile("global_load_dwordx4 %0, %1, %2 offset:%3 nt" : "=v"(d) : "v"(voff), "s"(base), "i"(OFF) : "memory");
}
__device__ __forceinline__ void st_sc1(char* a, f32x4 v) { asm volatile("global_store_dwordx4 %0, %1, off sc1\n\ts_nop 1" ::"v"(a), "v"(v) : "memory"); }
__device__ __forceinline__ void ld_sc1(f32x4& d, const char* a) { asm volatile("global_load_dwordx4 %0, %1, off sc1" : "=v"(d) : "v"(a) : "memory"); }
__device__ __forceinline__ void pin(bf16x8& d) { asm volatile("" : "+v"(d)); }
__device__ __forceinline__ void pin4(f32x4& d) { asm volatile("" : "+v"(d)); }
__device__ __forceinline__ void pinf(float& d) { asm volatile("" : "+v"(d)); }
__device__ __forceinline__ unsigned opq(unsigned d) { asm volatile("" : "+v"(d)); return d; }
typedef LDSP(char) lp;
__device__ __forceinline__ lp mkp(unsigned a) { return (lp)opq(a); }
__device__ __forceinline__ bf16x8 lds128(lp a) { return *(LDSP(bf16x8))a; }
__device__ __forceinline__ bf16x8 ldstr(lp a, int d) {
  bf16x4 lo = __builtin_amdgcn_ds_read_tr16_b64_v4i16((LDSP(bf16x4))a);
  bf16x4 hi = __builtin_amdgcn_ds_read_tr16_b64_v4i16((LDSP(bf16x4))(a + d));
  return __builtin_shufflevector(lo, hi, 0, 1, 2, 3, 4, 5, 6, 7);
}
__device__ __forceinline__ unsigned pk2(float a, float b) {
  f32x2 v = {a, b};
  return __builtin_bit_cast(unsigned, __builtin_convertvector(v, bf2));
}
__device__ __forceinline__ f32x4 mma(bf16x8 a, bf16x8 b, f32x4 c) {
  typedef __attribute__((ext_vector_type(8))) __bf16 b8;
  return __builtin_amdgcn_mfma_f32_16x16x32_bf16(__builtin_bit_cast(b8, a), __builtin_bit_cast(b8, b), c, 0, 0, 0);
}
__device__ __forceinline__ float xg_max(float v) {
  unsigned u = __float_as_uint(v);
  auto r = __builtin_amdgcn_permlane16_swap(u, u, false, false);
  v = fmaxf(__uint_as_float(r[0]), __uint_as_float(r[1]));
  u = __float_as_uint(v);
  auto s = __builtin_amdgcn_permlane32_swap(u, u, false, false);
  return fmaxf(__uint_as_float(s[0]), __uint_as_float(s[1]));
}
__device__ __forceinline__ float xg_sum(float v) {
  unsigned u = __float_as_uint(v);
  auto r = __builtin_amdgcn_permlane16_swap(u, u, false, false);
  v = __uint_as_float(r[0]) + __uint_as_float(r[1]);
  u = __float_as_uint(v);
  auto s = __builtin_amdgcn_permlane32_swap(u, u, false, false);
  return __uint_as_float(s[0]) + __uint_as_float(s[1]);
}

#define STAMP(k)
struct P1 {
  const char* x[2]; const char* k[2]; const char* v[2]; const char* wq[2];
  unsigned* cnt;
  char* ofr;
};

template <int NQ, int QT0, int HALF>
__device__ __forceinline__ void k1_body(const P1& p, int w, int lane, int br, int h0, int b) {
  const int g = lane >> 4, c = lane & 15, dq = w & 3;
  unsigned offW[3], offV[3], offX[3];
  sfor<0, 3>([&](auto I) {
    constexpr int pp = I.v;
    int o = 1024 * pp + 16 * lane, rr = o / 384, ch = (o % 384) >> 4;
    int hh = ((rr >> 1) & 1) | ((w & 1) << 1);
    int sc = (((ch >> 1) ^ hh) << 1) | (ch & 1);
    offV[pp] = (8 * w + rr) * 384 + sc * 16;
    offW[pp] = (8 * w + rr) * (HD * 2) + sc * 16;
    int row = 18 * w + 8 * pp + (lane >> 3);
    offX[pp] = row * (DM * 2) + (((lane & 7) ^ ((row >> 1) & 7)) << 4);
  });
  const unsigned ldsW = RING + w * 3072, ldsX = w * 2304;
  const unsigned offK = (16 * w + c) * 384 + g * 16;
  lp adT[3];
  int ut[3];
  sfor<0, 3>([&](auto I) {
    constexpr int t = I.v;
    ut[t] = 3 * dq + ((dq & 1) ? (t + 1) % 3 : t);
    int q4 = c >> 2, hh = ((q4 >> 1) & 1) | ((g & 1) << 1);
    adT[t] = mkp(RING + (8 * g + q4) * 384 + ((ut[t] ^ hh) << 5) + ((c & 3) << 3));
  });
  lp adX[2];
  adX[0] = mkp((QT0 * 16 + c) * 128 + (((0 + g) ^ ((c >> 1) & 7)) << 4));
  adX[1] = mkp((QT0 * 16 + c) * 128 + (((4 + g) ^ ((c >> 1) & 7)) << 4));
  const lp adL = mkp(lane * 16);
  const unsigned adH = ((g >> 1) * 16 + c) * 16 + (g & 1) * 8;
  const float CS = 0.07216878364870322f * 1.4426950408889634f;

  auto dW = [&](const char* wb, int i, auto I) {
    dma16(offW[I.v], wb + (size_t)i * 64 * HD * 2, ldsW + (i & 3) * SLOT + I.v * 1024); };
  auto dX = [&](const char* xb, int i, auto I) {
    unsigned l = ldsX + ((i + 2) % 3) * XBUF + I.v * 1024;
    if constexpr (I.v < 2) dma16(offX[I.v], xb + i * 128, l);
    else if (lane < 16) dma16(offX[2], xb + i * 128, l);
  };
  auto dV = [&](const char* vb, int i, auto I) {
    dma16nt(offV[I.v], vb + i * SLOT, ldsW + (i & 3) * SLOT + I.v * 1024); };
  auto issueW = [&](const char* wb, int i) { sfor<0, 3>([&](auto I) { dW(wb, i, I); }); };
  auto issueX = [&](const char* xb, int i) { sfor<0, 3>([&](auto I) { dX(xb, i, I); }); };
  constexpr int NST = 2 * NQ, NM = 3 * NQ, NR = 3 + NQ;
  unsigned stv = 0;
  STAMP(0);

  const char* xb = (br ? p.x[1] : p.x[0]) + (size_t)b * SQ * DM * 2;
  const char* wb0 = (br ? p.wq[1] : p.wq[0]) + (size_t)h0 * DH * 2;
  const size_t kvo = (size_t)(b * NH + h0) * SK * DH * 2;
  const char* kb0 = (br ? p.k[1] : p.k[0]) + kvo;
  const char* vb0 = (br ? p.v[1] : p.v[0]) + kvo;
  {
    const char* wb = wb0;
    issueW(wb, 0); issueW(wb, 1); issueX(xb, 0); issueW(wb, 2); issueX(xb, 1); issueW(wb, 3); issueX(xb, 2);
    wait_vm<12>(); bar();
  }
#pragma unroll 1
  for (int u = 0; u < 2; ++u) {
    const char* wb = wb0 + u * (4 * DH * 2);
    const char* kb = kb0 + u * (4 * SK * DH * 2);
    const char* vb = vb0 + u * (4 * SK * DH * 2);
    STAMP(1 + u * 28);
    bf16x8 kf[4][6];
    bf16x8 fa[2][3], fb[2][NQ];
    f32x4 acc[3][NQ];
    auto rdT = [&](auto S, auto KS, auto T) {
      constexpr int i = S.v, ks = KS.v;
      if constexpr ((i & 3) == 3) fa[ks][T.v] = ldstr(mkp((unsigned)(size_t)adT[T.v] + 3 * SLOT) + ks * 12288, 1536);
      else fa[ks][T.v] = ldstr(adT[T.v] + (i & 3) * SLOT + ks * 12288, 1536);
    };
    auto seg = [&](auto KS, auto NI, auto&& item) {
      sfor<0, NM>([&](auto M) {
        constexpr int q = M.v / 3, t = M.v % 3;
        acc[t][q] = mma(fa[KS.v][t], fb[KS.v][q], acc[t][q]);
        __builtin_amdgcn_sched_barrier(0);
        constexpr int per = (NI.v + NM - 1) / NM, lo = M.v * per < NI.v ? M.v * per : NI.v, hi = lo + per < NI.v ? lo + per : NI.v;
        sfor<lo, hi>([&](auto K) { item(K); __builtin_amdgcn_sched_barrier(0); });
      });
    };
    {
      sfor<0, 3>([&](auto T) { sfor<0, NQ>([&](auto Q) { acc[T.v][Q.v] = f32x4{0, 0, 0, 0}; }); });
      auto rd1 = [&](auto S, auto KS, auto R) {
        if constexpr (R.v == 0) fb[KS.v][0] = lds128(adX[KS.v] + ((S.v + 2) % 3) * XBUF);
        else if constexpr (R.v <= 3) rdT(S, KS, IC<R.v - 1>{});
        else fb[KS.v][R.v - 3] = lds128(adX[KS.v] + ((S.v + 2) % 3) * XBUF + (R.v - 3) * 2048);
      };
      auto vm = [&](auto J, auto N) {
        constexpr int j = J.v, n = N.v;
        constexpr int nx = j <= 6 ? 3 : 0, nw = j <= 5 ? 3 : 0;
        if constexpr (j <= 7) {
          if constexpr (n < nx) dX(xb, j + 3, IC<n>{});
          else if constexpr (n < nx + nw) dW(wb, j + 4, IC<n - nx>{});
          else { constexpr int m = 3 * j + n - nx - nw, pr = m / 12, s = (m % 12) / 2, jt = 2 * pr + (m & 1);
                 gload_nt<s * 64>(kf[jt][s], offK, kb + jt * 49152); }
        } else if constexpr (j == 8) dV(vb, 0, N);
        else dV(vb, 1 + n / 3, IC<n % 3>{});
      };
      sfor<0, NR>([&](auto R) { rd1(IC<0>{}, IC<0>{}, R); });
      __builtin_amdgcn_sched_barrier(0);
      sfor<0, 10>([&](auto S) {
        constexpr int i = S.v;
        constexpr int pv = i == 0 ? 0 : (i - 1 <= 5 ? 9 : i - 1 == 6 ? 6 : 3), p1 = NM - NR < pv ? NM - NR : pv;
        seg(IC<0>{}, IC<NR + pv - p1>{}, [&](auto K) {
          if constexpr (K.v < NR) rd1(S, IC<1>{}, K); else vm(IC<i - 1>{}, IC<p1 + K.v - NR>{});
        });
        if constexpr (i == 0) { if (u == 0) wait_vm<6>(); else wait_vm<6 + NST>(); }
        else if constexpr (i == 1) { if (u == 0) wait_vm<9>(); else wait_vm<9 + NST>(); }
        else if constexpr (i <= 6) wait_vm<15>();
        else if constexpr (i == 7) wait_vm<12>();
        else if constexpr (i == 8) wait_vm<6>();
        bar();
        STAMP(2 + i + u * 28);
        constexpr int cv = i <= 5 ? 9 : i == 6 ? 6 : i == 9 ? 9 : 3;
        if constexpr (i < 9) {
          constexpr int c1 = NM - NR < cv ? NM - NR : cv;
          seg(IC<1>{}, IC<NR + c1>{}, [&](auto K) {
            if constexpr (K.v < NR) rd1(IC<i + 1>{}, IC<0>{}, K); else vm(S, IC<K.v - NR>{});
          });
        } else seg(IC<1>{}, IC<cv>{}, [&](auto K) { vm(S, K); });
      });
      sfor<0, 3>([&](auto T) {
        lp a = mkp(adH + (ut[T.v] >> 1) * 1024 + (ut[T.v] & 1) * 512) + QT0 * 6144;
        sfor<0, NQ>([&](auto Q) {
          f32x4 v = acc[T.v][Q.v];
          *(LDSP(u32x2))(a + Q.v * 6144) = u32x2{pk2(v[0], v[1]), pk2(v[2], v[3])};
        });
      });
      bar();
    }
    STAMP(12 + u * 28);
    f32x4 sa[4][9];
    float mx[9];
    const lp adS = mkp((opq(lane) & 15) * 32 + STATM), adSw = mkp((opq(lane) & 15) * 32 + STATM + w * 4), adSg = mkp((opq(lane) & 15) * 32 + STATM + g * 8);
    {
      constexpr int PF = 3;
      bf16x8 qf[PF + 1];
      auto rowmax = [&](auto PR, auto Q) {
        f32x4 a = sa[2 * PR.v][Q.v], bb = sa[2 * PR.v + 1][Q.v];
        float m = fmaxf(fmaxf(fmaxf(a[0], a[1]), fmaxf(a[2], a[3])), fmaxf(fmaxf(bb[0], bb[1]), fmaxf(bb[2], bb[3])));
        if constexpr (PR.v == 0) mx[Q.v] = m;
        else { m = xg_max(fmaxf(mx[Q.v], m)); if (g == 0) *(LDSP(float))(adSw + Q.v * 512) = m; }
      };
      sfor<0, PF>([&](auto N) { qf[N.v] = lds128(adL + N.v * 1024); });
      sfor<0, 6>([&](auto S) { pin(kf[0][S.v]); pin(kf[1][S.v]); });
      __builtin_amdgcn_sched_barrier(0);
      sfor<0, 108>([&](auto N) {
        constexpr int n = N.v, pr = n / 54, q = (n % 54) / 6, s = n % 6;
        if constexpr (n == 54) {
          STAMP(13 + u * 28);
          wait_vm<12>();
          STAMP(14 + u * 28);
          sfor<0, 6>([&](auto S) { pin(kf[2][S.v]); pin(kf[3][S.v]); });
          __builtin_amdgcn_sched_barrier(0);
        }
        if constexpr (s == 0) { sa[2 * pr][q] = f32x4{0, 0, 0, 0}; sa[2 * pr + 1][q] = f32x4{0, 0, 0, 0}; }
        sa[2 * pr][q] = mma(kf[2 * pr][s], qf[n % (PF + 1)], sa[2 * pr][q]);
        __builtin_amdgcn_sched_barrier(0);
        if constexpr (n + PF < 108) qf[(n + PF) % (PF + 1)] = lds128(adL + ((n + PF) % 54) * 1024);
        __builtin_amdgcn_sched_barrier(0);
        sa[2 * pr + 1][q] = mma(kf[2 * pr + 1][s], qf[n % (PF + 1)], sa[2 * pr + 1][q]);
        __builtin_amdgcn_sched_barrier(0);
        if constexpr (s == 2 && n >= 6) { constexpr int pn = n - 6; rowmax(IC<pn / 54>{}, IC<(pn % 54) / 6>{}); __builtin_amdgcn_sched_barrier(0); }
      });
      rowmax(IC<1>{}, IC<8>{});
    }
    STAMP(15 + u * 28);
    bar();
    STAMP(16 + u * 28);
    float sm[9];
    sfor<0, 9>([&](auto Q) {
      f32x2 v = *(LDSP(f32x2))(adSg + Q.v * 512);
      mx[Q.v] = -CS * xg_max(fmaxf(v[0], v[1]));
      sm[Q.v] = 0.f;
    });
    const lp adP = mkp(adH + (((w & 3) >> 1) * 9) * 1024 + (w & 1) * 512);
    auto exph = [&](auto J, auto K) {
      constexpr int j = J.v, pb = ((2 * j + HALF) % 3) * XBUF, q = K.v / 2;
      if constexpr ((K.v & 1) == 0) {
        sfor<0, 4>([&](auto E) { sa[j][q][E.v] = __builtin_amdgcn_exp2f(fmaf(sa[j][q][E.v], CS, mx[q])); });
      } else {
        f32x4 e = sa[j][q];
        sm[q] += (e[0] + e[1]) + (e[2] + e[3]);
        if constexpr (j == 3) { sm[q] = xg_sum(sm[q]); if (g == 0) *(LDSP(float))(adSw + (STATS - STATM) + q * 512) = sm[q]; }
        else pinf(sm[q]);
        *(LDSP(u32x2))(adP + pb + q * 1024) = u32x2{pk2(e[0], e[1]), pk2(e[2], e[3])};
      }
    };
    sfor<0, 18>([&](auto K) { exph(IC<0>{}, K); });
    __builtin_amdgcn_sched_barrier(0);
    STAMP(17 + u * 28);
    wait_vm<9>();
    bar();
    STAMP(18 + u * 28);
    {
      const char* wn = wb0 + 4 * DH * 2; const char* xn = xb;
      sfor<0, 3>([&](auto T) { sfor<0, NQ>([&](auto Q) { acc[T.v][Q.v] = f32x4{0, 0, 0, 0}; }); });
      auto rd1 = [&](auto S, auto KS, auto R) {
        if constexpr (R.v == 0) fb[KS.v][0] = lds128(adL + (S.v % 3) * XBUF + (KS.v * 9 + QT0) * 1024);
        else if constexpr (R.v <= 3) rdT(S, KS, IC<R.v - 1>{});
        else fb[KS.v][R.v - 3] = lds128(adL + (S.v % 3) * XBUF + (KS.v * 9 + QT0 + R.v - 3) * 1024);
      };
      auto vm = [&](auto J, auto N) {
        constexpr int j = J.v, n = N.v;
        if constexpr (j <= 3) dV(vb, j + 4, N);
        else if (u == 0) { if constexpr (n < 3) dW(wn, j - 4, N); else dX(xn, j - 5, IC<n - 3>{}); }
      };
      sfor<0, NR>([&](auto R) { rd1(IC<0>{}, IC<0>{}, R); });
      __builtin_amdgcn_sched_barrier(0);
      if constexpr (HALF == 0) { sfor<0, 8>([&](auto K) { exph(IC<1>{}, K); }); __builtin_amdgcn_sched_barrier(0); }
      sfor<0, 8>([&](auto S) {
        constexpr int i = S.v;
        constexpr bool e0 = (i <= 5) && (((i + 1) & 1) != HALF);
        constexpr int j0 = (i + 2 - HALF) / 2;
        seg(IC<0>{}, IC<NR + (e0 ? 10 : 0)>{}, [&](auto K) {
          if constexpr (!e0) rd1(S, IC<1>{}, K);
          else if constexpr (K.v < 2 * NR) { if constexpr (K.v & 1) exph(IC<j0>{}, IC<8 + K.v / 2>{}); else rd1(S, IC<1>{}, IC<K.v / 2>{}); }
          else exph(IC<j0>{}, IC<8 + K.v - NR>{});
        });
        if constexpr (i <= 4) wait_vm<6>();
        else if constexpr (i == 5) { if (u == 0) wait_vm<6>(); else wait_vm<3>(); }
        else if constexpr (i == 6) { if (u == 0) wait_vm<9>(); else wait_vm<0>(); }
        else { if (u == 0) wait_vm<6>(); }
        bar();
        STAMP(19 + i + u * 28);
        constexpr bool e1 = (i <= 4) && ((i & 1) != HALF);
        constexpr int j1 = (i + 3 - HALF) / 2, cv = i <= 4 ? 3 : 6, nr = i < 7 ? NR : 0;
        seg(IC<1>{}, IC<nr + cv + (e1 ? 8 : 0)>{}, [&](auto K) {
          if constexpr (!e1) { if constexpr (K.v < nr) rd1(IC<i + 1>{}, IC<0>{}, K); else vm(S, IC<K.v - nr>{}); }
          else if constexpr (K.v < 2 * NR) { if constexpr (K.v & 1) exph(IC<j1>{}, IC<K.v / 2>{}); else rd1(IC<i + 1>{}, IC<0>{}, IC<K.v / 2>{}); }
          else if constexpr (K.v < NR + 8) exph(IC<j1>{}, IC<K.v - NR>{});
          else vm(S, IC<K.v - NR - 8>{});
        });
      });
      char* ob = p.ofr + ((size_t)((br * 8 + b) * 192 + (h0 + 4 * u) * 6) * 9 + QT0) * 1024 + opq(lane * 16);
      const int sf = ut[0] >> 1, sh = ut[2] >> 1, hp = ut[2] & 1;
      sfor<0, NQ>([&](auto Q) {
        f32x4 s0 = *(LDSP(f32x4))(adS + (STATS - STATM) + (QT0 + Q.v) * 512), s1 = *(LDSP(f32x4))(adS + (STATS - STATM) + (QT0 + Q.v) * 512 + 16);
        float r = __builtin_amdgcn_rcpf(((s0[0] + s0[1]) + (s0[2] + s0[3])) + ((s1[0] + s1[1]) + (s1[2] + s1[3])));
        f32x4 a0 = acc[0][Q.v] * r, a1 = acc[1][Q.v] * r, a2 = acc[2][Q.v] * r;
        *(u32x4*)(ob + (sf * 9 + Q.v) * 1024) = u32x4{pk2(a0[0], a0[1]), pk2(a0[2], a0[3]), pk2(a1[0], a1[1]), pk2(a1[2], a1[3])};
        *(u32x2*)(ob + (sh * 9 + Q.v) * 1024 + hp * 8) = u32x2{pk2(a2[0], a2[1]), pk2(a2[2], a2[3])};
      });
      __builtin_amdgcn_sched_barrier(0);
      STAMP(27 + u * 28);
    }
  }
}

__global__ __launch_bounds__(512, 2) void k1_attn(P1 p) {
  extern __shared__ __attribute__((aligned(16))) char smem[];
  const int w = __builtin_amdgcn_readfirstlane(threadIdx.x >> 6), lane = threadIdx.x & 63;
  const int xcd = blockIdx.x & 7, idx = blockIdx.x >> 3;
  const int br = xcd >> 2, h0 = 8 * (xcd & 3) + (idx & 3), b = idx >> 2;
  if (threadIdx.x == 0 && blockIdx.x < 64) p.cnt[blockIdx.x * 32] = 0u;
  if (w < 4) k1_body<5, 0, 0>(p, w, lane, br, h0, b);
  else k1_body<4, 5, 1>(p, w, lane, br, h0, b);
}

constexpr int OBUF = 38912, OWO = 18432, LDS2 = 4 * OBUF;
struct P2 {
  const char* ofr; const char* wo[2]; char* slab; unsigned* cnt; char* out;
};
template <int NQ, int QT0, int NN>
__device__ __forceinline__ void k2_body(const P2& p, int w, int lane, int n0, int br, int b, int nt, int ksp) {
  const int g = lane >> 4, c = lane & 15;
  unsigned offW[3];
  sfor<0, 3>([&](auto I) {
    int o = 1024 * I.v + 16 * lane, rr = o / 320, ch = (o % 320) >> 4;
    offW[I.v] = (8 * w + rr) * 1280 + (((((ch >> 1) ^ ((rr >> 2) & 1)) << 1) | (ch & 1)) << 4);
  });
  const unsigned offO = 2304 * w + 16 * lane;
  const char* ob = p.ofr + (size_t)((br * 8 + b) * 192 + ksp * 48) * 9216;
  const char* wb = (br ? p.wo[1] : p.wo[0]) + (size_t)ksp * 1536 * 1280 + nt * 320;
  auto vm = [&](int i, auto BUF, auto N) {
    constexpr int n = N.v;
    if constexpr (n < 3) {
      unsigned l = BUF.v * OBUF + 2304 * w + n * 1024;
      if constexpr (n < 2) dma16(offO, ob + i * OWO + n * 1024, l);
      else if (lane < 16) dma16(offO, ob + i * OWO + n * 1024, l);
    } else {
      unsigned l = BUF.v * OBUF + OWO + 2560 * w + (n - 3) * 1024;
      if constexpr (n < 5) dma16(offW[n - 3], wb + i * 64 * 1280, l);
      else if (lane < 32) dma16(offW[2], wb + i * 64 * 1280, l);
    }
  };
  const unsigned rowp = OWO + (4 * g + (c >> 2)) * 320 + (c & 3) * 8 + n0 * 32;
  const unsigned aT0 = rowp + ((n0 & 1) ? -(g & 1) * 32 : (g & 1) * 32), aT1 = rowp + ((n0 & 1) ? (g & 1) * 32 : -(g & 1) * 32);
  const unsigned aO = QT0 * 1024 + lane * 16;
  constexpr int NM = NN * NQ, NR = NN + NQ;
  bf16x8 fa[2][NN], fb[2][NQ];
  f32x4 acc[NM];
  sfor<0, NM>([&](auto M) { acc[M.v] = f32x4{0, 0, 0, 0}; });
  lp pO, pT0, pT1;
  auto setp = [&](int bufi) { pO = mkp(aO + bufi * OBUF); pT0 = mkp(aT0 + bufi * OBUF); pT1 = mkp(aT1 + bufi * OBUF); };
  auto rd1 = [&](auto KS, auto R) {
    constexpr int ks = KS.v, r = R.v;
    if constexpr (r == 0) fb[ks][0] = lds128(pO + ks * 9216);
    else if constexpr (r <= NN) fa[ks][r - 1] = ldstr((((r - 1) & 1) ? pT1 : pT0) + (r - 1) * 32 + ks * 10240, 5120);
    else fb[ks][r - NN] = lds128(pO + ks * 9216 + (r - NN) * 1024);
  };
  auto seg = [&](auto KS, auto NI, auto&& item) {
    sfor<0, NM>([&](auto M) {
      acc[M.v] = mma(fa[KS.v][M.v % NN], fb[KS.v][M.v / NN], acc[M.v]);
      __builtin_amdgcn_sched_barrier(0);
      constexpr int per = (NI.v + NM - 1) / NM, lo = M.v * per < NI.v ? M.v * per : NI.v, hi = lo + per < NI.v ? lo + per : NI.v;
      sfor<lo, hi>([&](auto K) { item(K); __builtin_amdgcn_sched_barrier(0); });
    });
  };
  unsigned stv = 0;
  STAMP(0);
  sfor<0, 4>([&](auto S) { sfor<0, 6>([&](auto N) { vm(S.v, S, N); }); });
  wait_vm<18>(); bar();
  setp(0);
  sfor<0, NR>([&](auto R) { rd1(IC<0>{}, R); });
  __builtin_amdgcn_sched_barrier(0);
  auto stage = [&](int i, auto J, auto LAST) {
    constexpr int j = J.v;
    constexpr int pw = (LAST.v && j > 0) ? 0 : 3;
    seg(IC<0>{}, IC<NR + pw>{}, [&](auto K) {
      if constexpr (K.v < NR) rd1(IC<1>{}, K);
      else if (i > 0) vm(i + 3, IC<(j + 3) & 3>{}, IC<3 + K.v - NR>{});
    });
    if constexpr (!LAST.v || j == 0) wait_vm<12>(); else if constexpr (j == 1) wait_vm<6>(); else if constexpr (j == 2) wait_vm<0>();
    bar();
    STAMP(1 + i);
    constexpr bool fin = LAST.v && j == 3;
    if constexpr (!fin) setp((j + 1) & 3);
    seg(IC<1>{}, IC<(fin ? 0 : NR) + (LAST.v ? 0 : 3)>{}, [&](auto K) {
      if constexpr (K.v < NR) rd1(IC<0>{}, K); else vm(i + 4, J, IC<K.v - NR>{});
    });
  };
#pragma unroll 1
  for (int it = 0; it < 5; ++it) sfor<0, 4>([&](auto J) { stage(4 * it + J.v, J, IC<0>{}); });
  sfor<0, 4>([&](auto J) { stage(20 + J.v, J, IC<1>{}); });
  STAMP(25);
  const int grp = (br * 8 + b) * 4 + nt;
  char* sl = p.slab + ((size_t)(grp * 4 * 8 + w) * 16) * 1024 + lane * 16;
  f32x4 r[4][3];
  auto tail = [&](auto KSP) {
    constexpr int ks = KSP.v;
    sfor<0, NM>([&](auto M) { if constexpr ((M.v & 3) != ks) st_sc1(sl + (ks * 128 + M.v) * 1024, acc[M.v]); });
    STAMP(26);
    wait_vm<0>();
    bar();
    STAMP(27);
    if (w == 0) {
      if (lane == 0) {
        __hip_atomic_fetch_add(p.cnt + grp * 32, 1u, __ATOMIC_RELAXED, __HIP_MEMORY_SCOPE_AGENT);
        while (__hip_atomic_load(p.cnt + grp * 32, __ATOMIC_RELAXED, __HIP_MEMORY_SCOPE_AGENT) < 4u) __builtin_amdgcn_s_sleep(1);
      }
    }
    bar();
    STAMP(28);
    sfor<0, NM>([&](auto M) {
      if constexpr ((M.v & 3) == ks) sfor<0, 3>([&](auto O) { ld_sc1(r[M.v / 4][O.v], sl + ((O.v < ks ? O.v : O.v + 1) * 128 + M.v) * 1024); });
    });
    __builtin_amdgcn_sched_barrier(0);
    wait_vm<0>();
    sfor<0, NM>([&](auto M) {
      if constexpr ((M.v & 3) == ks) {
        sfor<0, 3>([&](auto O) { pin4(r[M.v / 4][O.v]); });
        f32x4 v[4];
        sfor<0, 4>([&](auto K) { if constexpr (K.v == ks) v[K.v] = acc[M.v]; else v[K.v] = r[M.v / 4][K.v < ks ? K.v : K.v - 1]; });
        f32x4 o = (v[0] + v[1]) + (v[2] + v[3]);
        *(u32x2*)(p.out + ((size_t)(b * 144 + 16 * (QT0 + M.v / NN) + c) * 1280 + br * 640 + nt * 160 + 16 * (n0 + M.v % NN) + 4 * g) * 2) =
            u32x2{pk2(o[0], o[1]), pk2(o[2], o[3])};
      }
    });
  };
  if (ksp == 0) tail(IC<0>{}); else if (ksp == 1) tail(IC<1>{}); else if (ksp == 2) tail(IC<2>{}); else tail(IC<3>{});
  STAMP(29);
}
__global__ __launch_bounds__(512, 2) void k2_oproj(P2 p) {
  extern __shared__ __attribute__((aligned(16))) char smem[];
  const int w = __builtin_amdgcn_readfirstlane(threadIdx.x >> 6), lane = threadIdx.x & 63;
  const int xcd = blockIdx.x & 7, idx = blockIdx.x >> 3;
  const int br = xcd >> 2, b = idx >> 2, nt = idx & 3, ksp = xcd & 3;
  if (w < 2) k2_body<5, 0, 3>(p, w, lane, 3 * w, br, b, nt, ksp);
  else if (w < 4) k2_body<5, 0, 2>(p, w, lane, 2 * w + 2, br, b, nt, ksp);
  else if (w < 6) k2_body<4, 5, 2>(p, w, lane, 2 * w - 8, br, b, nt, ksp);
  else k2_body<4, 5, 3>(p, w, lane, 3 * w - 14, br, b, nt, ksp);
}

extern "C" void kernel_launch(void* const* d_in, const int* in_sizes, int n_in, void* d_out, int out_size, void* d_ws,
                              size_t ws_size, hipStream_t stream) {
  P1 p;
  p.x[0] = (const char*)d_in[0]; p.k[0] = (const char*)d_in[1]; p.v[0] = (const char*)d_in[2]; p.wq[0] = (const char*)d_in[3];
  p.x[1] = (const char*)d_in[5]; p.k[1] = (const char*)d_in[6]; p.v[1] = (const char*)d_in[7]; p.wq[1] = (const char*)d_in[8];
  p.ofr = (char*)d_ws;
  p.cnt = (unsigned*)((char*)d_ws + (30u << 20));
  P2 q;
  q.ofr = (const char*)d_ws; q.wo[0] = (const char*)d_in[4]; q.wo[1] = (const char*)d_in[9];
  q.slab = (char*)d_ws + (32u << 20); q.cnt = p.cnt; q.out = (char*)d_out;
  (void)hipFuncSetAttribute((const void*)k1_attn, hipFuncAttributeMaxDynamicSharedMemorySize, LDS1);
  (void)hipFuncSetAttribute((const void*)k2_oproj, hipFuncAttributeMaxDynamicSharedMemorySize, LDS2);
  k1_attn<<<256, 512, LDS1, stream>>>(p);
  k2_oproj<<<256, 512, LDS2, stream>>>(q);
}
```

```cpp
#pragma clang diagnostic ignored "-Wint-to-pointer-cast"
#include <hip/hip_runtime.h>
#include <hip/hip_bf16.h>
#include <stdint.h>

typedef __attribute__((ext_vector_type(8))) short bf16x8;
typedef __attribute__((ext_vector_type(4))) short bf16x4;
typedef __attribute__((ext_vector_type(4))) float f32x4;
typedef __attribute__((ext_vector_type(2))) float f32x2;
typedef __attribute__((ext_vector_type(2))) __bf16 bf2;
typedef __attribute__((ext_vector_type(2))) unsigned u32x2;
typedef __attribute__((ext_vector_type(4))) unsigned u32x4;
#define LDSP(T) __attribute__((address_space(3))) T*

constexpr int SQ = 144, DM = 640, NH = 32, SK = 512, DH = 192, HD = 6144;
constexpr int XBUF = 18432, SLOT = 24576, RING = 55296, STATM = 153600, STATS = 158208, LDS1 = 162816;

template <int I> struct IC { static constexpr int v = I; };
template <int B, int E, class F> __device__ __forceinline__ void sfor(F&& f) {
  if constexpr (B < E) { f(IC<B>{}); sfor<B + 1, E>(f); }
}
template <int N> __device__ __forceinline__ void wait_vm() { asm volatile("s_waitcnt vmcnt(%0)" ::"i"(N) : "memory"); }
__device__ __forceinline__ void bar() {
  __builtin_amdgcn_sched_barrier(0);
  asm volatile("s_waitcnt lgkmcnt(0)\n\ts_barrier" ::: "memory");
  __builtin_amdgcn_sched_barrier(0);
}
__device__ __forceinline__ void dma16(unsigned voff, const void* base, unsigned lds) {
  asm volatile("s_mov_b32 m0, %1\n\ts_nop 0\n\tglobal_load_lds_dwordx4 %0, %2" ::"v"(voff), "s"(lds), "s"(base) : "memory");
}
__device__ __forceinline__ void dma16nt(unsigned voff, const void* base, unsigned lds) {
  asm volatile("s_mov_b32 m0, %1\n\ts_nop 0\n\tglobal_load_lds_dwordx4 %0, %2 nt" ::"v"(voff), "s"(lds), "s"(base) : "memory");
}
template <int OFF> __device__ __forceinline__ void gload_nt(bf16x8& d, unsigned voff, const void* base) {
  asm volatile("global_load_dwordx4 %0, %1, %2 offset:%3 nt" : "=v"(d) : "v"(voff), "s"(base), "i"(OFF) : "memory");
}
template <int OFF> __device__ __forceinline__ void gload_nt_if(bf16x8& d, unsigned voff, const void* base, int sk) {
  asm volatile("s_cmp_lg_u32 %4, 0\n\ts_cbranch_scc1 .Lk%=\n\tglobal_load_dwordx4 %0, %1, %2 offset:%3 nt\n.Lk%=:"
               : "=v"(d) : "v"(voff), "s"(base), "i"(OFF), "s"(sk) : "memory", "scc");
}
__device__ __forceinline__ void st_sc1(char* a, f32x4 v) { asm volatile("global_store_dwordx4 %0, %1, off sc1\n\ts_nop 1" ::"v"(a), "v"(v) : "memory"); }
__device__ __forceinline__ void ld_sc1(f32x4& d, const char* a) { asm volatile("global_load_dwordx4 %0, %1, off sc1" : "=v"(d) : "v"(a) : "memory"); }
__device__ __forceinline__ void pin(bf16x8& d) { asm volatile("" : "+v"(d)); }
__device__ __forceinline__ void pin4(f32x4& d) { asm volatile("" : "+v"(d)); }
__device__ __forceinline__ void pinf(float& d) { asm volatile("" : "+v"(d)); }
__device__ __forceinline__ unsigned opq(unsigned d) { asm volatile("" : "+v"(d)); return d; }
__device__ __forceinline__ unsigned lid() {
  unsigned d; asm volatile("v_mbcnt_lo_u32_b32 %0, -1, 0\n\tv_mbcnt_hi_u32_b32 %0, -1, %0" : "=v"(d)); return d;
}
typedef LDSP(char) lp;
__device__ __forceinline__ lp mkp(unsigned a) { return (lp)opq(a); }
__device__ __forceinline__ bf16x8 lds128(lp a) { return *(LDSP(bf16x8))a; }
__device__ __forceinline__ bf16x8 ldstr(lp a, int d) {
  bf16x4 lo = __builtin_amdgcn_ds_read_tr16_b64_v4i16((LDSP(bf16x4))a);
  bf16x4 hi = __builtin_amdgcn_ds_read_tr16_b64_v4i16((LDSP(bf16x4))(a + d));
  return __builtin_shufflevector(lo, hi, 0, 1, 2, 3, 4, 5, 6, 7);
}
__device__ __forceinline__ unsigned pk2(float a, float b) {
  f32x2 v = {a, b};
  return __builtin_bit_cast(unsigned, __builtin_convertvector(v, bf2));
}
__device__ __forceinline__ f32x4 mma(bf16x8 a, bf16x8 b, f32x4 c) {
  typedef __attribute__((ext_vector_type(8))) __bf16 b8;
  return __builtin_amdgcn_mfma_f32_16x16x32_bf16(__builtin_bit_cast(b8, a), __builtin_bit_cast(b8, b), c, 0, 0, 0);
}
__device__ __forceinline__ void kfix(bf16x8& a, bf16x8& b) {
  u32x4 x = __builtin_bit_cast(u32x4, a), y = __builtin_bit_cast(u32x4, b), p, q;
  for (int i = 0; i < 4; ++i) {
    p[i] = __builtin_amdgcn_update_dpp(x[i], y[i], 0x128, 0xF, 0xC, false);
    q[i] = __builtin_amdgcn_update_dpp(y[i], x[i], 0x128, 0xF, 0x3, false);
  }
  a = __builtin_bit_cast(bf16x8, p); b = __builtin_bit_cast(bf16x8, q);
}
__device__ __forceinline__ float max3(float a, float b, float c) { float d; asm("v_max3_f32 %0, %1, %2, %3" : "=v"(d) : "v"(a), "v"(b), "v"(c)); return d; }
template <int OFF> __device__ __forceinline__ void lds_max(lp a, float v) { asm volatile("ds_max_f32 %0, %1 offset:%2" ::"v"(a), "v"(v), "i"(OFF) : "memory"); }
__device__ __forceinline__ float bfly_sum(float a, float b, float c, float d) {
  auto r = __builtin_amdgcn_permlane16_swap(__float_as_uint(a), __float_as_uint(b), false, false);
  auto s = __builtin_amdgcn_permlane16_swap(__float_as_uint(c), __float_as_uint(d), false, false);
  float ab = __uint_as_float(r[0]) + __uint_as_float(r[1]), cd = __uint_as_float(s[0]) + __uint_as_float(s[1]);
  auto t = __builtin_amdgcn_permlane32_swap(__float_as_uint(ab), __float_as_uint(cd), false, false);
  return __uint_as_float(t[0]) + __uint_as_float(t[1]);
}
#define STAMP(k)
struct P1 {
  const char* x[2]; const char* k[2]; const char* v[2]; const char* wq[2];
  unsigned* cnt;
  char* ofr;
};

template <int NQ, int QT0, int HALF>
__device__ __forceinline__ void k1_body(const P1& p, int w, int lane, int br, int h0, int b) {
  const int dq = w & 3;
  unsigned offW[3], offV[3], offX[3], offK;
  auto mkWV = [&](unsigned* off, int rs) {
    const unsigned l = lid();
    sfor<0, 3>([&](auto I) {
      unsigned t = 64 * I.v + l, rr = (t * 2731u) >> 16, ch = t - 24 * rr;
      unsigned hh = ((rr >> 1) & 1) | ((w & 1) << 1);
      off[I.v] = (8 * w + rr) * rs + ((ch ^ (hh << 1)) << 4);
    });
  };
  auto mkX = [&]() {
    const unsigned l = lid();
    sfor<0, 3>([&](auto I) {
      unsigned row = 18 * w + 8 * I.v + (l >> 3);
      offX[I.v] = row * (DM * 2) + (((l & 7) ^ ((row >> 1) & 7)) << 4);
    });
  };
  auto mkK = [&]() { const unsigned l = lid(); offK = (16 * w + (l & 7)) * 384 + (l & 8) * 8 + (l & 48); };
  const unsigned ldsW = RING + w * 3072, ldsX = w * 2304;
  lp adT[3];
  int ut[3];
  sfor<0, 3>([&](auto I) { ut[I.v] = 3 * dq + ((dq & 1) ? (I.v + 1) % 3 : I.v); });
  auto mkT = [&]() {
    const unsigned l = lid(), q4 = (l >> 2) & 3, hh = (q4 >> 1) | ((l >> 3) & 2);
    sfor<0, 3>([&](auto I) { adT[I.v] = mkp(RING + (8 * (l >> 4) + q4) * 384 + ((ut[I.v] ^ hh) << 5) + ((l & 3) << 3)); });
  };
  lp adX[2];
  auto mkXr = [&]() {
    const unsigned l = lid(), cc = l & 15, gg = l >> 4;
    adX[0] = mkp((QT0 * 16 + cc) * 128 + ((gg ^ (cc >> 1)) << 4));
    adX[1] = mkp((QT0 * 16 + cc) * 128 + (((4 + gg) ^ (cc >> 1)) << 4));
  };
  auto mkH = [&]() { const unsigned l = lid(); return ((l >> 5) * 16 + (l & 15)) * 16 + ((l >> 1) & 8); };
  mkWV(offW, HD * 2); mkX(); mkK(); mkT();
  const lp adL = mkp(lane * 16);
  const float CS = 0.07216878364870322f * 1.4426950408889634f;

  auto dW = [&](const char* wb, int i, auto I) {
    dma16(offW[I.v], wb + (size_t)i * 64 * HD * 2, ldsW + (i & 3) * SLOT + I.v * 1024); };
  auto dX = [&](const char* xb, int i, auto I) {
    unsigned l = ldsX + ((i + 2) % 3) * XBUF + I.v * 1024;
    if constexpr (I.v < 2) dma16(offX[I.v], xb + i * 128, l);
    else if (lid() < 16) dma16(offX[2], xb + i * 128, l);
  };
  auto dV = [&](const char* vb, int i, auto I) {
    dma16nt(offV[I.v], vb + i * SLOT, ldsW + (i & 3) * SLOT + I.v * 1024); };
  auto issueW = [&](const char* wb, int i) { sfor<0, 3>([&](auto I) { dW(wb, i, I); }); };
  auto issueX = [&](const char* xb, int i) { sfor<0, 3>([&](auto I) { dX(xb, i, I); }); };
  constexpr int NST = 2 * NQ, NM = 3 * NQ, NR = 3 + NQ;
  unsigned stv = 0;
  STAMP(0);

  const char* xb = (br ? p.x[1] : p.x[0]) + (size_t)b * SQ * DM * 2;
  const char* wb0 = (br ? p.wq[1] : p.wq[0]) + (size_t)h0 * DH * 2;
  const size_t kvo = (size_t)(b * NH + h0) * SK * DH * 2;
  const char* kb0 = (br ? p.k[1] : p.k[0]) + kvo;
  const char* vb0 = (br ? p.v[1] : p.v[0]) + kvo;
  bf16x8 kf[4][6];
  auto dK = [&](const char* kb, auto PR, auto M) {
    constexpr int m = M.v, L = m / 4, jt = 2 * PR.v + ((m >> 1) & 1), hf = m & 1;
    gload_nt<L * 128 + hf * 3072>(kf[jt][2 * L + hf], offK, kb + jt * 49152);
  };
  auto dKn = [&](const char* kb, int sk, auto M) {
    constexpr int m = M.v, L = m / 4, jt = (m >> 1) & 1, hf = m & 1;
    gload_nt_if<L * 128 + hf * 3072>(kf[jt][2 * L + hf], offK, kb + jt * 49152, sk);
  };
  {
    const char* wb = wb0;
    issueW(wb, 0); issueW(wb, 1); issueX(xb, 0); issueW(wb, 2); issueX(xb, 1); issueW(wb, 3); issueX(xb, 2);
    sfor<0, 12>([&](auto M) { dK(kb0, IC<0>{}, M); });
    wait_vm<24>(); bar();
  }
#pragma unroll 1
  for (int u = 0; u < 2; ++u) {
    const char* wb = wb0 + u * (4 * DH * 2);
    const char* kb = kb0 + u * (4 * SK * DH * 2);
    const char* vb = vb0 + u * (4 * SK * DH * 2);
    STAMP(1 + u * 28);
    bf16x8 fa[2][3], fb[2][NQ];
    f32x4 acc[3][NQ];
    auto rdT = [&](auto S, auto KS, auto T) {
      constexpr int i = S.v, ks = KS.v;
      if constexpr ((i & 3) == 3) fa[ks][T.v] = ldstr(mkp((unsigned)(size_t)adT[T.v] + 3 * SLOT) + ks * 12288, 1536);
      else fa[ks][T.v] = ldstr(adT[T.v] + (i & 3) * SLOT + ks * 12288, 1536);
    };
    auto seg = [&](auto KS, auto NI, auto&& item) {
      sfor<0, NM>([&](auto M) {
        constexpr int q = M.v / 3, t = M.v % 3;
        acc[t][q] = mma(fa[KS.v][t], fb[KS.v][q], acc[t][q]);
        __builtin_amdgcn_sched_barrier(0);
        constexpr int per = (NI.v + NM - 1) / NM, lo = M.v * per < NI.v ? M.v * per : NI.v, hi = lo + per < NI.v ? lo + per : NI.v;
        sfor<lo, hi>([&](auto K) { item(K); __builtin_amdgcn_sched_barrier(0); });
      });
    };
    {
      sfor<0, 3>([&](auto T) { sfor<0, NQ>([&](auto Q) { acc[T.v][Q.v] = f32x4{0, 0, 0, 0}; }); });
      auto rd1 = [&](auto S, auto KS, auto R) {
        if constexpr (R.v == 0) fb[KS.v][0] = lds128(adX[KS.v] + ((S.v + 2) % 3) * XBUF);
        else if constexpr (R.v <= 3) rdT(S, KS, IC<R.v - 1>{});
        else fb[KS.v][R.v - 3] = lds128(adX[KS.v] + ((S.v + 2) % 3) * XBUF + (R.v - 3) * 2048);
      };
      auto vm = [&](auto J, auto N) {
        constexpr int j = J.v, n = N.v;
        if constexpr (j <= 5) { if constexpr (n < 3) dX(xb, j + 3, N); else dW(wb, j + 4, IC<n - 3>{}); }
        else if constexpr (j == 6) { if constexpr (n < 3) dX(xb, 9, N); else dK(kb, IC<1>{}, IC<n - 3>{}); }
        else if constexpr (j == 7) dK(kb, IC<1>{}, IC<6 + n>{});
        else if constexpr (j == 8) dV(vb, 0, N);
        else dV(vb, 1 + n / 3, IC<n % 3>{});
      };
      mkXr();
      sfor<0, NR>([&](auto R) { rd1(IC<0>{}, IC<0>{}, R); });
      __builtin_amdgcn_sched_barrier(0);
      sfor<0, 10>([&](auto S) {
        constexpr int i = S.v;
        constexpr int pv = i == 0 ? 0 : (i - 1 == 6 ? 9 : i - 1 == 8 ? 3 : 6), p1 = NM - NR < pv ? NM - NR : pv;
        constexpr int nf = i == 9 ? 3 : 0;
        seg(IC<0>{}, IC<NR + pv - p1 + nf>{}, [&](auto K) {
          if constexpr (K.v < NR) rd1(S, IC<1>{}, K);
          else if constexpr (K.v < NR + pv - p1) vm(IC<i - 1>{}, IC<p1 + K.v - NR>{});
          else { constexpr int L = K.v - (NR + pv - p1); kfix(kf[1][2 * L], kf[1][2 * L + 1]); pin(kf[1][2 * L]); pin(kf[1][2 * L + 1]); }
        });
        if constexpr (i <= 1) { if (u == 0) wait_vm<18>(); else wait_vm<6 + NST>(); }
        else if constexpr (i <= 6) wait_vm<9>();
        else if constexpr (i <= 8) wait_vm<12>();
        bar();
        STAMP(2 + i + u * 28);
        if constexpr (i == 6) mkK();
        if constexpr (i == 8) { mkWV(offV, 384); sfor<0, 6>([&](auto S2) { pin(kf[0][S2.v]); pin(kf[1][S2.v]); }); __builtin_amdgcn_sched_barrier(0); }
        constexpr int cv = (i == 6 || i == 9) ? 9 : i == 8 ? 3 : 6;
        if constexpr (i < 9) {
          constexpr int c1 = NM - NR < cv ? NM - NR : cv;
          seg(IC<1>{}, IC<NR + c1 + (i == 8 ? 3 : 0)>{}, [&](auto K) {
            if constexpr (K.v < NR) rd1(IC<i + 1>{}, IC<0>{}, K);
            else if constexpr (K.v < NR + c1) vm(S, IC<K.v - NR>{});
            else { constexpr int L = K.v - NR - c1; kfix(kf[0][2 * L], kf[0][2 * L + 1]); pin(kf[0][2 * L]); pin(kf[0][2 * L + 1]); }
          });
        } else seg(IC<1>{}, IC<cv>{}, [&](auto K) { vm(S, K); });
      });
      if (w < 3) *(LDSP(float))(mkp(STATM + w * 256 + (lid() << 2))) = -__builtin_inff();
      const unsigned adH = mkH();
      sfor<0, 3>([&](auto T) {
        lp a = mkp(adH + (ut[T.v] >> 1) * 1024 + (ut[T.v] & 1) * 512) + QT0 * 6144;
        sfor<0, NQ>([&](auto Q) {
          f32x4 v = acc[T.v][Q.v];
          *(LDSP(u32x2))(a + Q.v * 6144) = u32x2{pk2(v[0], v[1]), pk2(v[2], v[3])};
        });
      });
      bar();
    }
    STAMP(12 + u * 28);
    f32x4 sa[4][9];
    float mx[9];
    const lp adM = mkp((lid() & 15) * 48 + STATM);
    {
      constexpr int PF = 4;
      bf16x8 qf[PF + 1];
      auto rowmax = [&](auto PR, auto Q) {
        f32x4 a = sa[2 * PR.v][Q.v], bb = sa[2 * PR.v + 1][Q.v];
        float m = max3(max3(a[0], a[1], a[2]), max3(a[3], bb[0], bb[1]), bb[2]);
        if constexpr (PR.v == 0) mx[Q.v] = max3(m, bb[3], bb[3]);
        else lds_max<Q.v * 4>(adM, max3(m, bb[3], mx[Q.v]));
      };
      sfor<0, PF>([&](auto N) { qf[N.v] = lds128(adL + N.v * 1024); });
      __builtin_amdgcn_sched_barrier(0);
      sfor<0, 108>([&](auto N) {
        constexpr int n = N.v, pr = n / 54, q = (n % 54) / 6, s = n % 6;
        if constexpr (n == 54) {
          STAMP(13 + u * 28);
          wait_vm<12>();
          STAMP(14 + u * 28);
          sfor<0, 6>([&](auto S) { pin(kf[2][S.v]); pin(kf[3][S.v]); });
          __builtin_amdgcn_sched_barrier(0);
        }
        if constexpr (n == 54 || n == 56 || n == 58) {
          kfix(kf[2][s], kf[2][s + 1]); kfix(kf[3][s], kf[3][s + 1]);
          __builtin_amdgcn_sched_barrier(0);
        }
        if constexpr (s == 0) { sa[2 * pr][q] = f32x4{0, 0, 0, 0}; sa[2 * pr + 1][q] = f32x4{0, 0, 0, 0}; }
        sa[2 * pr][q] = mma(kf[2 * pr][s], qf[n % (PF + 1)], sa[2 * pr][q]);
        __builtin_amdgcn_sched_barrier(0);
        if constexpr (n + PF < 108) qf[(n + PF) % (PF + 1)] = lds128(adL + ((n + PF) % 54) * 1024);
        __builtin_amdgcn_sched_barrier(0);
        sa[2 * pr + 1][q] = mma(kf[2 * pr + 1][s], qf[n % (PF + 1)], sa[2 * pr + 1][q]);
        __builtin_amdgcn_sched_barrier(0);
        if constexpr (s == 2 && n >= 6) { constexpr int pn = n - 6; rowmax(IC<pn / 54>{}, IC<(pn % 54) / 6>{}); __builtin_amdgcn_sched_barrier(0); }
      });
      rowmax(IC<1>{}, IC<8>{});
    }
    STAMP(15 + u * 28);
    bar();
    STAMP(16 + u * 28);
    float sm[9];
    {
      f32x4 m0 = *(LDSP(f32x4))(adM), m1 = *(LDSP(f32x4))(adM + 16);
      float m2 = *(LDSP(float))(adM + 32);
      sfor<0, 4>([&](auto Q) { mx[Q.v] = -CS * m0[Q.v]; mx[4 + Q.v] = -CS * m1[Q.v]; });
      mx[8] = -CS * m2;
    }
    const lp adP = mkp(mkH() + (((w & 3) >> 1) * 9) * 1024 + (w & 1) * 512);
    auto exph = [&](auto J, auto K) {
      constexpr int j = J.v, pb = ((2 * j + HALF) % 3) * XBUF, q = K.v / 2;
      if constexpr (K.v >= 18) {
        constexpr int k = K.v - 18;
        float t;
        if constexpr (k < 2) t = bfly_sum(sm[4 * k], sm[4 * k + 1], sm[4 * k + 2], sm[4 * k + 3]);
        else t = bfly_sum(sm[8], sm[8], sm[8], sm[8]);
        unsigned l = lid();
        *(LDSP(float))(mkp(STATS + (l & 15) * 32 + (k < 2 ? (l >> 4) * 512 : 0) + w * 4) + k * 2048) = t;
      } else if constexpr ((K.v & 1) == 0) {
        f32x4 v = sa[j][q];
        f32x2 c2 = {CS, CS}, m2 = {mx[q], mx[q]};
        f32x2 lo = __builtin_elementwise_fma(f32x2{v[0], v[1]}, c2, m2), hi = __builtin_elementwise_fma(f32x2{v[2], v[3]}, c2, m2);
        sa[j][q] = f32x4{__builtin_amdgcn_exp2f(lo[0]), __builtin_amdgcn_exp2f(lo[1]), __builtin_amdgcn_exp2f(hi[0]), __builtin_amdgcn_exp2f(hi[1])};
      } else {
        f32x4 e = sa[j][q];
        f32x2 t = f32x2{e[0], e[1]} + f32x2{e[2], e[3]};
        if constexpr (j == 0) sm[q] = t[0] + t[1]; else sm[q] = (sm[q] + t[0]) + t[1];
        pinf(sm[q]);
        *(LDSP(u32x2))(adP + pb + q * 1024) = u32x2{pk2(e[0], e[1]), pk2(e[2], e[3])};
      }
    };
    sfor<0, 18>([&](auto K) { exph(IC<0>{}, K); });
    __builtin_amdgcn_sched_barrier(0);
    mkT(); mkWV(offV, 384);
    STAMP(17 + u * 28);
    wait_vm<9>();
    bar();
    STAMP(18 + u * 28);
    {
      const char* wn = wb0 + 4 * DH * 2; const char* xn = xb;
      sfor<0, 3>([&](auto T) { sfor<0, NQ>([&](auto Q) { acc[T.v][Q.v] = f32x4{0, 0, 0, 0}; }); });
      auto rd1 = [&](auto S, auto KS, auto R) {
        if constexpr (R.v == 0) fb[KS.v][0] = lds128(adL + (S.v % 3) * XBUF + (KS.v * 9 + QT0) * 1024);
        else if constexpr (R.v <= 3) rdT(S, KS, IC<R.v - 1>{});
        else fb[KS.v][R.v - 3] = lds128(adL + (S.v % 3) * XBUF + (KS.v * 9 + QT0 + R.v - 3) * 1024);
      };
      const char* kn = kb0 + 4 * SK * DH * 2;
      auto vm = [&](auto J, auto N) {
        constexpr int j = J.v, n = N.v;
        if constexpr (j <= 3 && n < 3) dV(vb, j + 4, N);
        else if constexpr (j == 2) dKn(kn, u, IC<n - 3>{});
        else if constexpr (j == 4 && n >= 3) dKn(kn, u, IC<n + 3>{});
        else if (u == 0) { if constexpr (n < 3) dW(wn, j - 4, N); else dX(xn, j - 5, IC<n - 3>{}); }
      };
      float rs[NQ];
      auto rsum = [&](auto Q) {
        const lp adS = mkp((lid() & 15) * 32 + STATS + QT0 * 512);
        f32x4 s0 = *(LDSP(f32x4))(adS + Q.v * 512), s1 = *(LDSP(f32x4))(adS + Q.v * 512 + 16);
        s0 += s1;
        rs[Q.v] = __builtin_amdgcn_rcpf((s0[0] + s0[1]) + (s0[2] + s0[3]));
        pinf(rs[Q.v]);
      };
      auto pvseg = [&](auto SG, auto NRD, auto&& rd, auto NV, auto&& ex) {
        constexpr int t = SG.v + 5 - 2 * HALF, j = t / 4, pt = t % 4;
        constexpr bool on = t >= 4 && t < 16;
        constexpr int e0 = pt == 0 ? 0 : pt == 1 ? 5 : pt == 2 ? 9 : 14, e1 = pt == 0 ? 5 : pt == 1 ? 9 : pt == 2 ? 14 : (j == 3 ? 21 : 18);
        constexpr int ne = on ? e1 - e0 : 0, nrd = NRD.v, mn = ne < nrd ? ne : nrd;
        auto item = [&](auto K) {
          constexpr int k = K.v;
          if constexpr (k < 2 * mn) { if constexpr (k & 1) exph(IC<j>{}, IC<e0 + k / 2>{}); else rd(IC<k / 2>{}); }
          else if constexpr (k < mn + nrd) rd(IC<k - mn>{});
          else if constexpr (k < ne + nrd) exph(IC<j>{}, IC<e0 + k - nrd>{});
          else ex(IC<k - ne - nrd>{});
        };
        if constexpr (SG.v < 0) sfor<0, nrd + ne>([&](auto K) { item(IC<(K.v < nrd - mn ? 2 * mn + K.v : K.v - (nrd - mn))>{}); __builtin_amdgcn_sched_barrier(0); });
        else seg(IC<(SG.v & 1)>{}, IC<nrd + ne + NV.v>{}, item);
      };
      if constexpr (HALF == 0) {
        sfor<0, NR>([&](auto R) { rd1(IC<0>{}, IC<0>{}, R); });
        __builtin_amdgcn_sched_barrier(0);
        pvseg(IC<-1>{}, IC<0>{}, [&](auto) {}, IC<0>{}, [&](auto) {});
      } else {
        sfor<0, NR>([&](auto R) { rd1(IC<0>{}, IC<0>{}, R); });
        __builtin_amdgcn_sched_barrier(0);
      }
      sfor<0, 8>([&](auto S) {
        constexpr int i = S.v;
        pvseg(IC<2 * i>{}, IC<NR>{}, [&](auto R) { rd1(S, IC<1>{}, R); }, IC<(i == 7 ? NQ : 0)>{}, rsum);
        if constexpr (i <= 2) wait_vm<6>();
        else if constexpr (i <= 4) { if (u == 0) wait_vm<12>(); else wait_vm<6>(); }
        else if constexpr (i == 5) { if (u == 0) wait_vm<18>(); else wait_vm<3>(); }
        else if constexpr (i == 6) { if (u == 0) wait_vm<15>(); else wait_vm<0>(); }
        else { if (u == 0) wait_vm<6>(); }
        bar();
        STAMP(19 + i + u * 28);
        if constexpr (i == 2) mkK();
        if constexpr (i == 4) mkWV(offW, HD * 2);
        if constexpr (i == 5) mkX();
        constexpr int cv = (i == 2 || i == 4) ? 9 : i <= 3 ? 3 : 6;
        pvseg(IC<2 * i + 1>{}, IC<(i < 7 ? NR : 0)>{}, [&](auto R) { rd1(IC<i + 1>{}, IC<0>{}, R); }, IC<cv>{}, [&](auto K) { vm(S, K); });
      });
      char* ob = p.ofr + ((size_t)((br * 8 + b) * 192 + (h0 + 4 * u) * 6) * 9 + QT0) * 1024 + (lid() << 4);
      const int sf = ut[0] >> 1, sh = ut[2] >> 1, hp = ut[2] & 1;
      sfor<0, NQ>([&](auto Q) {
        float r = rs[Q.v];
        f32x4 a0 = acc[0][Q.v] * r, a1 = acc[1][Q.v] * r, a2 = acc[2][Q.v] * r;
        *(u32x4*)(ob + (sf * 9 + Q.v) * 1024) = u32x4{pk2(a0[0], a0[1]), pk2(a0[2], a0[3]), pk2(a1[0], a1[1]), pk2(a1[2], a1[3])};
        *(u32x2*)(ob + (sh * 9 + Q.v) * 1024 + hp * 8) = u32x2{pk2(a2[0], a2[1]), pk2(a2[2], a2[3])};
      });
      __builtin_amdgcn_sched_barrier(0);
      STAMP(27 + u * 28);
    }
  }
}

__global__ __launch_bounds__(512, 2) void k1_attn(P1 p) {
  extern __shared__ __attribute__((aligned(16))) char smem[];
  const int w = __builtin_amdgcn_readfirstlane(threadIdx.x >> 6), lane = threadIdx.x & 63;
  const int xcd = blockIdx.x & 7, idx = blockIdx.x >> 3;
  const int br = xcd >> 2, h0 = 8 * (xcd & 3) + (idx & 3), b = idx >> 2;
  if (threadIdx.x == 0 && blockIdx.x < 64) p.cnt[blockIdx.x * 32] = 0u;
  if (w < 4) k1_body<5, 0, 0>(p, w, lane, br, h0, b);
  else k1_body<4, 5, 1>(p, w, lane, br, h0, b);
}

constexpr int OBUF = 38912, OWO = 18432, LDS2 = 4 * OBUF;
struct P2 {
  const char* ofr; const char* wo[2]; char* slab; unsigned* cnt; char* out;
};
template <int NQ, int QT0, int NN>
__device__ __forceinline__ void k2_body(const P2& p, int w, int lane, int n0, int br, int b, int nt, int ksp) {
  const int g = lane >> 4, c = lane & 15;
  unsigned offW[3];
  sfor<0, 3>([&](auto I) {
    int o = 1024 * I.v + 16 * lane, rr = o / 320, ch = (o % 320) >> 4;
    offW[I.v] = (8 * w + rr) * 1280 + (((((ch >> 1) ^ ((rr >> 2) & 1)) << 1) | (ch & 1)) << 4);
  });
  const unsigned offO = 2304 * w + 16 * lane;
  const char* ob = p.ofr + (size_t)((br * 8 + b) * 192 + ksp * 48) * 9216;
  const char* wb = (br ? p.wo[1] : p.wo[0]) + (size_t)ksp * 1536 * 1280 + nt * 320;
  auto vm = [&](int i, auto BUF, auto N) {
    constexpr int n = N.v;
    if constexpr (n < 3) {
      unsigned l = BUF.v * OBUF + 2304 * w + n * 1024;
      if constexpr (n < 2) dma16(offO, ob + i * OWO + n * 1024, l);
      else if (lane < 16) dma16(offO, ob + i * OWO + n * 1024, l);
    } else {
      unsigned l = BUF.v * OBUF + OWO + 2560 * w + (n - 3) * 1024;
      if constexpr (n < 5) dma16(offW[n - 3], wb + i * 64 * 1280, l);
      else if (lane < 32) dma16(offW[2], wb + i * 64 * 1280, l);
    }
  };
  const unsigned rowp = OWO + (4 * g + (c >> 2)) * 320 + (c & 3) * 8 + n0 * 32;
  const unsigned aT0 = rowp + ((n0 & 1) ? -(g & 1) * 32 : (g & 1) * 32), aT1 = rowp + ((n0 & 1) ? (g & 1) * 32 : -(g & 1) * 32);
  const unsigned aO = QT0 * 1024 + lane * 16;
  constexpr int NM = NN * NQ, NR = NN + NQ;
  bf16x8 fa[2][NN], fb[2][NQ];
  f32x4 acc[NM];
  sfor<0, NM>([&](auto M) { acc[M.v] = f32x4{0, 0, 0, 0}; });
  lp pO, pT0, pT1;
  auto setp = [&](int bufi) { pO = mkp(aO + bufi * OBUF); pT0 = mkp(aT0 + bufi * OBUF); pT1 = mkp(aT1 + bufi * OBUF); };
  auto rd1 = [&](auto KS, auto R) {
    constexpr int ks = KS.v, r = R.v;
    if constexpr (r == 0) fb[ks][0] = lds128(pO + ks * 9216);
    else if constexpr (r <= NN) fa[ks][r - 1] = ldstr((((r - 1) & 1) ? pT1 : pT0) + (r - 1) * 32 + ks * 10240, 5120);
    else fb[ks][r - NN] = lds128(pO + ks * 9216 + (r - NN) * 1024);
  };
  auto seg = [&](auto KS, auto NI, auto&& item) {
    sfor<0, NM>([&](auto M) {
      acc[M.v] = mma(fa[KS.v][M.v % NN], fb[KS.v][M.v / NN], acc[M.v]);
      __builtin_amdgcn_sched_barrier(0);
      constexpr int per = (NI.v + NM - 1) / NM, lo = M.v * per < NI.v ? M.v * per : NI.v, hi = lo + per < NI.v ? lo + per : NI.v;
      sfor<lo, hi>([&](auto K) { item(K); __builtin_amdgcn_sched_barrier(0); });
    });
  };
  unsigned stv = 0;
  STAMP(0);
  sfor<0, 4>([&](auto S) { sfor<0, 6>([&](auto N) { vm(S.v, S, N); }); });
  wait_vm<18>(); bar();
  setp(0);
  sfor<0, NR>([&](auto R) { rd1(IC<0>{}, R); });
  __builtin_amdgcn_sched_barrier(0);
  auto stage = [&](int i, auto J, auto LAST) {
    constexpr int j = J.v;
    constexpr int pw = (LAST.v && j > 0) ? 0 : 3;
    seg(IC<0>{}, IC<NR + pw>{}, [&](auto K) {
      if constexpr (K.v < NR) rd1(IC<1>{}, K);
      else if (i > 0) vm(i + 3, IC<(j + 3) & 3>{}, IC<3 + K.v - NR>{});
    });
    if constexpr (!LAST.v || j == 0) wait_vm<12>(); else if constexpr (j == 1) wait_vm<6>(); else if constexpr (j == 2) wait_vm<0>();
    bar();
    STAMP(1 + i);
    constexpr bool fin = LAST.v && j == 3;
    if constexpr (!fin) setp((j + 1) & 3);
    seg(IC<1>{}, IC<(fin ? 0 : NR) + (LAST.v ? 0 : 3)>{}, [&](auto K) {
      if constexpr (K.v < NR) rd1(IC<0>{}, K); else vm(i + 4, J, IC<K.v - NR>{});
    });
  };
#pragma unroll 1
  for (int it = 0; it < 5; ++it) sfor<0, 4>([&](auto J) { stage(4 * it + J.v, J, IC<0>{}); });
  sfor<0, 4>([&](auto J) { stage(20 + J.v, J, IC<1>{}); });
  STAMP(25);
  const int grp = (br * 8 + b) * 4 + nt;
  char* sl = p.slab + ((size_t)(grp * 4 * 8 + w) * 16) * 1024 + lane * 16;
  f32x4 r[4][3];
  auto tail = [&](auto KSP) {
    constexpr int ks = KSP.v;
    sfor<0, NM>([&](auto M) { if constexpr ((M.v & 3) != ks) st_sc1(sl + (ks * 128 + M.v) * 1024, acc[M.v]); });
    STAMP(26);
    wait_vm<0>();
    bar();
    STAMP(27);
    if (w == 0) {
      if (lane == 0) {
        __hip_atomic_fetch_add(p.cnt + grp * 32, 1u, __ATOMIC_RELAXED, __HIP_MEMORY_SCOPE_AGENT);
        while (__hip_atomic_load(p.cnt + grp * 32, __ATOMIC_RELAXED, __HIP_MEMORY_SCOPE_AGENT) < 4u) __builtin_amdgcn_s_sleep(1);
      }
    }
    bar();
    STAMP(28);
    sfor<0, NM>([&](auto M) {
      if constexpr ((M.v & 3) == ks) sfor<0, 3>([&](auto O) { ld_sc1(r[M.v / 4][O.v], sl + ((O.v < ks ? O.v : O.v + 1) * 128 + M.v) * 1024); });
    });
    __builtin_amdgcn_sched_barrier(0);
    wait_vm<0>();
    sfor<0, NM>([&](auto M) {
      if constexpr ((M.v & 3) == ks) {
        sfor<0, 3>([&](auto O) { pin4(r[M.v / 4][O.v]); });
        f32x4 v[4];
        sfor<0, 4>([&](auto K) { if constexpr (K.v == ks) v[K.v] = acc[M.v]; else v[K.v] = r[M.v / 4][K.v < ks ? K.v : K.v - 1]; });
        f32x4 o = (v[0] + v[1]) + (v[2] + v[3]);
        *(u32x2*)(p.out + ((size_t)(b * 144 + 16 * (QT0 + M.v / NN) + c) * 1280 + br * 640 + nt * 160 + 16 * (n0 + M.v % NN) + 4 * g) * 2) =
            u32x2{pk2(o[0], o[1]), pk2(o[2], o[3])};
      }
    });
  };
  if (ksp == 0) tail(IC<0>{}); else if (ksp == 1) tail(IC<1>{}); else if (ksp == 2) tail(IC<2>{}); else tail(IC<3>{});
  STAMP(29);
}
__global__ __launch_bounds__(512, 2) void k2_oproj(P2 p) {
  extern __shared__ __attribute__((aligned(16))) char smem[];
  const int w = __builtin_amdgcn_readfirstlane(threadIdx.x >> 6), lane = threadIdx.x & 63;
  const int xcd = blockIdx.x & 7, idx = blockIdx.x >> 3;
  const int br = xcd >> 2, b = idx >> 2, nt = idx & 3, ksp = xcd & 3;
  if (w < 2) k2_body<5, 0, 3>(p, w, lane, 3 * w, br, b, nt, ksp);
  else if (w < 4) k2_body<5, 0, 2>(p, w, lane, 2 * w + 2, br, b, nt, ksp);
  else if (w < 6) k2_body<4, 5, 2>(p, w, lane, 2 * w - 8, br, b, nt, ksp);
  else k2_body<4, 5, 3>(p, w, lane, 3 * w - 14, br, b, nt, ksp);
}

extern "C" void kernel_launch(void* const* d_in, const int* in_sizes, int n_in, void* d_out, int out_size, void* d_ws,
                              size_t ws_size, hipStream_t stream) {
  P1 p;
  p.x[0] = (const char*)d_in[0]; p.k[0] = (const char*)d_in[1]; p.v[0] = (const char*)d_in[2]; p.wq[0] = (const char*)d_in[3];
  p.x[1] = (const char*)d_in[5]; p.k[1] = (const char*)d_in[6]; p.v[1] = (const char*)d_in[7]; p.wq[1] = (const char*)d_in[8];
  p.ofr = (char*)d_ws;
  p.cnt = (unsigned*)((char*)d_ws + (30u << 20));
  P2 q;
  q.ofr = (const char*)d_ws; q.wo[0] = (const char*)d_in[4]; q.wo[1] = (const char*)d_in[9];
  q.slab = (char*)d_ws + (32u << 20); q.cnt = p.cnt; q.out = (char*)d_out;
  (void)hipFuncSetAttribute((const void*)k1_attn, hipFuncAttributeMaxDynamicSharedMemorySize, LDS1);
  (void)hipFuncSetAttribute((const void*)k2_oproj, hipFuncAttributeMaxDynamicSharedMemorySize, LDS2);
  k1_attn<<<256, 512, LDS1, stream>>>(p);
  k2_oproj<<<256, 512, LDS2, stream>>>(q);
}
```

```cpp
#pragma clang diagnostic ignored "-Wint-to-pointer-cast"
#include <hip/hip_runtime.h>
#include <hip/hip_bf16.h>
#include <stdint.h>

typedef __attribute__((ext_vector_type(8))) short bf16x8;
typedef __attribute__((ext_vector_type(4))) short bf16x4;
typedef __attribute__((ext_vector_type(4))) float f32x4;
typedef __attribute__((ext_vector_type(2))) float f32x2;
typedef __attribute__((ext_vector_type(2))) __bf16 bf2;
typedef __attribute__((ext_vector_type(2))) unsigned u32x2;
typedef __attribute__((ext_vector_type(4))) unsigned u32x4;
#define LDSP(T) __attribute__((address_space(3))) T*

constexpr int SQ = 144, DM = 640, NH = 32, SK = 512, DH = 192, HD = 6144;
constexpr int XBUF = 18432, SLOT = 24576, RING = 55296, STATM = 153600, STATS = 158208, LDS1 = 162816;

template <int I> struct IC { static constexpr int v = I; };
template <int B, int E, class F> __device__ __forceinline__ void sfor(F&& f) {
  if constexpr (B < E) { f(IC<B>{}); sfor<B + 1, E>(f); }
}
template <int N> __device__ __forceinline__ void wait_vm() { asm volatile("s_waitcnt vmcnt(%0)" ::"i"(N) : "memory"); }
__device__ __forceinline__ void bar() {
  __builtin_amdgcn_sched_barrier(0);
  asm volatile("s_waitcnt lgkmcnt(0)\n\ts_barrier" ::: "memory");
  __builtin_amdgcn_sched_barrier(0);
}
__device__ __forceinline__ void dma16(unsigned voff, const void* base, unsigned lds) {
  asm volatile("s_mov_b32 m0, %1\n\ts_nop 0\n\tglobal_load_lds_dwordx4 %0, %2" ::"v"(voff), "s"(lds), "s"(base) : "memory");
}
__device__ __forceinline__ void dma16nt(unsigned voff, const void* base, unsigned lds) {
  asm volatile("s_mov_b32 m0, %1\n\ts_nop 0\n\tglobal_load_lds_dwordx4 %0, %2 nt" ::"v"(voff), "s"(lds), "s"(base) : "memory");
}
template <int OFF> __device__ __forceinline__ void gload_nt_if(bf16x8& d, unsigned voff, const void* base, int sk) {
  asm volatile("s_cmp_lg_u32 %4, 0\n\ts_cbranch_scc1 .Lk%=\n\tglobal_load_dwordx4 %0, %1, %2 offset:%3 nt\n.Lk%=:"
               : "=v"(d) : "v"(voff), "s"(base), "i"(OFF), "s"(sk) : "memory", "scc");
}
template <int OFF> __device__ __forceinline__ void gload_nt_keep(bf16x8& d, unsigned voff, const void* base, int sk) {
  asm volatile("s_cmp_lg_u32 %4, 0\n\ts_cbranch_scc1 .Lk%=\n\tglobal_load_dwordx4 %0, %1, %2 offset:%3 nt\n.Lk%=:"
               : "+v"(d) : "v"(voff), "s"(base), "i"(OFF), "s"(sk) : "memory", "scc");
}
__device__ __forceinline__ void undef8(bf16x8& d) { asm volatile("" : "=v"(d)); }
__device__ __forceinline__ void dma4(unsigned voff, const void* base, unsigned lds) {
  asm volatile("s_mov_b32 m0, %1\n\ts_nop 0\n\tglobal_load_lds_dword %0, %2" ::"v"(voff), "s"(lds), "s"(base) : "memory");
}
__device__ __forceinline__ void st_sc1(char* a, f32x4 v) { asm volatile("global_store_dwordx4 %0, %1, off sc1\n\ts_nop 1" ::"v"(a), "v"(v) : "memory"); }
__device__ __forceinline__ void ld_sc1(f32x4& d, const char* a) { asm volatile("global_load_dwordx4 %0, %1, off sc1" : "=v"(d) : "v"(a) : "memory"); }
__device__ __forceinline__ void pin(bf16x8& d) { asm volatile("" : "+v"(d)); }
__device__ __forceinline__ void pin4(f32x4& d) { asm volatile("" : "+v"(d)); }
__device__ __forceinline__ void pinf(float& d) { asm volatile("" : "+v"(d)); }
__device__ __forceinline__ unsigned opq(unsigned d) { asm volatile("" : "+v"(d)); return d; }
__device__ __forceinline__ unsigned lid() {
  unsigned d; asm volatile("v_mbcnt_lo_u32_b32 %0, -1, 0\n\tv_mbcnt_hi_u32_b32 %0, -1, %0" : "=v"(d)); return d;
}
typedef LDSP(char) lp;
__device__ __forceinline__ lp mkp(unsigned a) { return (lp)opq(a); }
__device__ __forceinline__ bf16x8 lds128(lp a) { return *(LDSP(bf16x8))a; }
__device__ __forceinline__ bf16x8 ldstr(lp a, int d) {
  bf16x4 lo = __builtin_amdgcn_ds_read_tr16_b64_v4i16((LDSP(bf16x4))a);
  bf16x4 hi = __builtin_amdgcn_ds_read_tr16_b64_v4i16((LDSP(bf16x4))(a + d));
  return __builtin_shufflevector(lo, hi, 0, 1, 2, 3, 4, 5, 6, 7);
}
__device__ __forceinline__ unsigned pk2(float a, float b) {
  f32x2 v = {a, b};
  return __builtin_bit_cast(unsigned, __builtin_convertvector(v, bf2));
}
__device__ __forceinline__ f32x4 mma(bf16x8 a, bf16x8 b, f32x4 c) {
  typedef __attribute__((ext_vector_type(8))) __bf16 b8;
  return __builtin_amdgcn_mfma_f32_16x16x32_bf16(__builtin_bit_cast(b8, a), __builtin_bit_cast(b8, b), c, 0, 0, 0);
}
__device__ __forceinline__ void kfix(bf16x8& a, bf16x8& b) {
  u32x4 x = __builtin_bit_cast(u32x4, a), y = __builtin_bit_cast(u32x4, b), p, q;
  for (int i = 0; i < 4; ++i) {
    p[i] = __builtin_amdgcn_update_dpp(x[i], y[i], 0x128, 0xF, 0xC, false);
    q[i] = __builtin_amdgcn_update_dpp(y[i], x[i], 0x128, 0xF, 0x3, false);
  }
  a = __builtin_bit_cast(bf16x8, p); b = __builtin_bit_cast(bf16x8, q);
}
__device__ __forceinline__ float max3(float a, float b, float c) { float d; asm("v_max3_f32 %0, %1, %2, %3" : "=v"(d) : "v"(a), "v"(b), "v"(c)); return d; }
template <int OFF> __device__ __forceinline__ void lds_max(lp a, float v) { asm volatile("ds_max_f32 %0, %1 offset:%2" ::"v"(a), "v"(v), "i"(OFF) : "memory"); }
__device__ __forceinline__ float bfly_sum(float a, float b, float c, float d) {
  auto r = __builtin_amdgcn_permlane16_swap(__float_as_uint(a), __float_as_uint(b), false, false);
  auto s = __builtin_amdgcn_permlane16_swap(__float_as_uint(c), __float_as_uint(d), false, false);
  float ab = __uint_as_float(r[0]) + __uint_as_float(r[1]), cd = __uint_as_float(s[0]) + __uint_as_float(s[1]);
  auto t = __builtin_amdgcn_permlane32_swap(__float_as_uint(ab), __float_as_uint(cd), false, false);
  return __uint_as_float(t[0]) + __uint_as_float(t[1]);
}
#define STAMP(k)
struct P1 {
  const char* x[2]; const char* k[2]; const char* v[2]; const char* wq[2];
  const char* wo[2];
  unsigned* cnt;
  char* ofr;
};

#define EARLYX 0
template <int NQ, int QT0, int HALF>
__device__ __forceinline__ void k1_body(const P1& p, int w, int lane, int br, int h0, int b) {
  const int dq = w & 3;
  unsigned offW[3], offV[3], offX[3], offK;
  auto mkWV = [&](unsigned* off, int rs) {
    const unsigned l = lid();
    sfor<0, 3>([&](auto I) {
      unsigned t = 64 * I.v + l, rr = (t * 2731u) >> 16, ch = t - 24 * rr;
      unsigned hh = ((rr >> 1) & 1) | ((w & 1) << 1);
      off[I.v] = (8 * w + rr) * rs + ((ch ^ (hh << 1)) << 4);
    });
  };
  auto mkX = [&]() {
    const unsigned l = lid();
    sfor<0, 3>([&](auto I) {
      unsigned row = 18 * w + 8 * I.v + (l >> 3);
      offX[I.v] = row * (DM * 2) + (((l & 7) ^ ((row >> 1) & 7)) << 4);
    });
  };
  auto mkK = [&]() { const unsigned l = lid(); offK = (16 * w + (l & 7)) * 384 + (l & 8) * 8 + (l & 48); };
  const unsigned ldsW = RING + w * 3072, ldsX = w * 2304;
  lp adT[3];
  int ut[3];
  sfor<0, 3>([&](auto I) { ut[I.v] = 3 * dq + ((dq & 1) ? (I.v + 1) % 3 : I.v); });
  auto mkT = [&]() {
    const unsigned l = lid(), q4 = (l >> 2) & 3, hh = (q4 >> 1) | ((l >> 3) & 2);
    sfor<0, 3>([&](auto I) { adT[I.v] = mkp(RING + (8 * (l >> 4) + q4) * 384 + ((ut[I.v] ^ hh) << 5) + ((l & 3) << 3)); });
  };
  lp adX[2];
  auto mkXr = [&]() {
    const unsigned l = lid(), cc = l & 15, gg = l >> 4;
    adX[0] = mkp((QT0 * 16 + cc) * 128 + ((gg ^ (cc >> 1)) << 4));
    adX[1] = mkp((QT0 * 16 + cc) * 128 + (((4 + gg) ^ (cc >> 1)) << 4));
  };
  auto mkH = [&]() { const unsigned l = lid(); return ((l >> 5) * 16 + (l & 15)) * 16 + ((l >> 1) & 8); };
  mkWV(offW, HD * 2); mkX(); mkK(); mkT();
  const lp adL = mkp(lane * 16);
  const float CS = 0.07216878364870322f * 1.4426950408889634f;

  auto dW = [&](const char* wb, int i, auto I) {
    dma16(offW[I.v], wb + (size_t)i * 64 * HD * 2, ldsW + (i & 3) * SLOT + I.v * 1024); };
  auto dX = [&](const char* xb, int i, auto I) {
    unsigned l = ldsX + ((i + 2) % 3) * XBUF + I.v * 1024;
    if constexpr (I.v < 2) dma16(offX[I.v], xb + i * 128, l);
    else if (lid() < 16) dma16(offX[2], xb + i * 128, l);
  };
  auto dV = [&](const char* vb, int i, auto I) {
    dma16nt(offV[I.v], vb + i * SLOT, ldsW + (i & 3) * SLOT + I.v * 1024); };
  auto issueW = [&](const char* wb, int i) { sfor<0, 3>([&](auto I) { dW(wb, i, I); }); };
  auto issueX = [&](const char* xb, int i) { sfor<0, 3>([&](auto I) { dX(xb, i, I); }); };
  constexpr int NST = 2 * NQ, NM = 3 * NQ, NR = 3 + NQ;
  unsigned stv = 0;
  STAMP(0);

  const char* xb = (br ? p.x[1] : p.x[0]) + (size_t)b * SQ * DM * 2;
  const char* wb0 = (br ? p.wq[1] : p.wq[0]) + (size_t)h0 * DH * 2;
  const size_t kvo = (size_t)(b * NH + h0) * SK * DH * 2;
  const char* kb0 = (br ? p.k[1] : p.k[0]) + kvo;
  const char* vb0 = (br ? p.v[1] : p.v[0]) + kvo;
  bf16x8 kf[4][6];
  auto dKn = [&](const char* kb, int sk, auto M) {
    constexpr int m = M.v, L = m / 4, jt = (m >> 1) & 1, hf = m & 1;
    gload_nt_if<L * 128 + hf * 3072>(kf[jt][2 * L + hf], offK, kb + jt * 49152, sk);
  };
  auto dK0 = [&](const char* kb, int sk, auto M) {
    constexpr int m = M.v, L = m < 12 ? m / 4 : (m % 6) / 2, jt = m < 12 ? (m >> 1) & 1 : m / 6, hf = m & 1;
    if constexpr (m < 18) gload_nt_keep<L * 128 + hf * 3072>(kf[jt][2 * L + hf], offK, kb + jt * 49152, sk);
    else gload_nt_if<L * 128 + hf * 3072>(kf[jt][2 * L + hf], offK, kb + jt * 49152, 0);
  };
  const unsigned ldsT = STATM + 1024 + w * 256;
  auto touchW = [&](const char* wb) {
    unsigned l = lid(); l = l < 60 ? l : 59;
    unsigned r = (l * 43u) >> 8;
    dma4((80 * b + 10 * w + r) * (HD * 2) + (l - 6 * r) * 64, wb, ldsT);
  };
  const int late = (b & 1) ^ EARLYX;
  touchW(wb0);
  { unsigned l = lid(); l = l < 45 ? l : 44;
    dma4((h0 & 3) * 46080 + (90 * w + l) * 64, xb, ldsT); dma4((h0 & 3) * 46080 + (90 * w + 45 + l) * 64, xb, ldsT); }
  sfor<0, 6>([&](auto S) { undef8(kf[0][S.v]); undef8(kf[1][S.v]); undef8(kf[2][S.v]); });
  sfor<0, 18>([&](auto M) { dK0(kb0, late, M); });
  issueW(wb0, 0); issueX(xb, 0); issueW(wb0, 1); issueX(xb, 1);
  wait_vm<6>(); bar();
#pragma unroll 1
  for (int u = 0; u < 2; ++u) {
    const char* wb = wb0 + u * (4 * DH * 2);
    const char* kb = kb0 + u * (4 * SK * DH * 2);
    const char* vb = vb0 + u * (4 * SK * DH * 2);
    STAMP(1 + u * 28);
    const int nk0 = u | (late ^ 1), nk1 = (u & late) ^ 1, nk2 = u ^ late ^ 1;
    bf16x8 fa[2][3], fb[2][NQ];
    f32x4 acc[3][NQ];
    auto rdT = [&](auto S, auto KS, auto T) {
      constexpr int i = S.v, ks = KS.v;
      if constexpr ((i & 3) == 3) fa[ks][T.v] = ldstr(mkp((unsigned)(size_t)adT[T.v] + 3 * SLOT) + ks * 12288, 1536);
      else fa[ks][T.v] = ldstr(adT[T.v] + (i & 3) * SLOT + ks * 12288, 1536);
    };
    auto seg = [&](auto KS, auto NA, auto&& ia, auto NB, auto&& ib, auto NC, auto&& ic) {
      sfor<0, NM>([&](auto M) {
        constexpr int m = M.v, q = m / 3, t = m % 3;
        acc[t][q] = mma(fa[KS.v][t], fb[KS.v][q], acc[t][q]);
        __builtin_amdgcn_sched_barrier(0);
        if constexpr (m < NA.v) { ia(M); __builtin_amdgcn_sched_barrier(0); }
        sfor<(m * NB.v + NM - 1) / NM, ((m + 1) * NB.v + NM - 1) / NM>([&](auto K) { ib(K); __builtin_amdgcn_sched_barrier(0); });
        sfor<(m * NC.v + NM / 2) / NM, ((m + 1) * NC.v + NM / 2) / NM>([&](auto K) { ic(K); __builtin_amdgcn_sched_barrier(0); });
      });
    };
    auto nop = [&](auto) {};
    {
      sfor<0, 3>([&](auto T) { sfor<0, NQ>([&](auto Q) { acc[T.v][Q.v] = f32x4{0, 0, 0, 0}; }); });
      auto rd1 = [&](auto S, auto KS, auto R) {
        if constexpr (R.v == 0) fb[KS.v][0] = lds128(adX[KS.v] + ((S.v + 2) % 3) * XBUF);
        else if constexpr (R.v <= 3) rdT(S, KS, IC<R.v - 1>{});
        else fb[KS.v][R.v - 3] = lds128(adX[KS.v] + ((S.v + 2) % 3) * XBUF + (R.v - 3) * 2048);
      };
      auto vm = [&](auto J, auto N) {
        constexpr int j = J.v, n = N.v;
        if constexpr (j < 0) { if (u == 0) { if constexpr (n < 3) dW(wb, 2, N); else if constexpr (n < 6) dX(xb, 2, IC<n - 3>{}); else dW(wb, 3, IC<n - 6>{}); } }
        else if constexpr (j <= 5) { if constexpr (n < 3) dX(xb, j + 3, N); else if constexpr (n < 6) dW(wb, j + 4, IC<n - 3>{}); else dK0(kb, n < 18 ? nk0 : nk1, IC<n - 6>{}); }
        else if constexpr (j == 6) { if constexpr (n < 3) dX(xb, 9, N); else dK0(kb, nk2, IC<12 + n - 3>{}); }
        else if constexpr (j == 7) dK0(kb, nk2, IC<16 + n>{});
        else dK0(kb, nk2, IC<18 + n>{});
      };
      mkXr();
      sfor<0, NR>([&](auto R) { rd1(IC<0>{}, IC<0>{}, R); });
      __builtin_amdgcn_sched_barrier(0);
      sfor<0, 10>([&](auto S) {
        constexpr int i = S.v;
        constexpr int pv = i == 0 ? 9 : i == 4 ? 24 : i <= 6 ? 6 : i == 7 ? 7 : i == 8 ? 2 : 3, p1 = i == 0 ? 0 : (pv + 1) / 2;
        seg(IC<0>{}, IC<NR>{}, [&](auto R) { rd1(S, IC<1>{}, R); }, IC<pv - p1>{}, [&](auto K) { vm(IC<i - 1>{}, IC<p1 + K.v>{}); },
            IC<(i == 9 ? 3 : 0)>{}, [&](auto L) { kfix(kf[1][2 * L.v], kf[1][2 * L.v + 1]); pin(kf[1][2 * L.v]); pin(kf[1][2 * L.v + 1]); });
        if constexpr (i == 0) { if (u == 0) wait_vm<9>(); else wait_vm<9 + NST>(); }
        else if constexpr (i == 1) { if (u == 0) wait_vm<9>(); else wait_vm<6 + NST>(); }
        else if constexpr (i == 4 || i == 5) { if (nk0 == 0) wait_vm<21>(); else if (nk1 == 0) wait_vm<15>(); else wait_vm<9>(); }
        else if constexpr (i <= 6) wait_vm<9>();
        else if constexpr (i == 7) { if (nk2 == 0) wait_vm<10>(); else wait_vm<6>(); }
        else if constexpr (i == 8) { if (nk2 == 0) wait_vm<6>(); else wait_vm<0>(); }
        bar();
        STAMP(2 + i + u * 28);
        if constexpr (i == 8) { sfor<0, 6>([&](auto S2) { pin(kf[0][S2.v]); pin(kf[1][S2.v]); }); __builtin_amdgcn_sched_barrier(0); }
        constexpr int cv = i == 3 ? 24 : i <= 5 ? 6 : i == 6 ? 7 : i == 7 ? 2 : i == 8 ? 3 : 0;
        seg(IC<1>{}, IC<(i < 9 ? NR : 0)>{}, [&](auto R) { rd1(IC<i + 1>{}, IC<0>{}, R); }, IC<(cv + 1) / 2>{}, [&](auto K) { vm(S, K); },
            IC<(i == 8 ? 3 : 0)>{}, [&](auto L) { kfix(kf[0][2 * L.v], kf[0][2 * L.v + 1]); pin(kf[0][2 * L.v]); pin(kf[0][2 * L.v + 1]); });
      });
      mkWV(offV, 384);
      if (w < 3) *(LDSP(float))(mkp(STATM + w * 256 + (lid() << 2))) = -__builtin_inff();
      const unsigned adH = mkH();
      sfor<0, 3>([&](auto T) {
        lp a = mkp(adH + (ut[T.v] >> 1) * 1024 + (ut[T.v] & 1) * 512) + QT0 * 6144;
        sfor<0, NQ>([&](auto Q) {
          f32x4 v = acc[T.v][Q.v];
          *(LDSP(u32x2))(a + Q.v * 6144) = u32x2{pk2(v[0], v[1]), pk2(v[2], v[3])};
        });
      });
      bar();
    }
    STAMP(12 + u * 28);
    f32x4 sa[4][9];
    float mx[9];
    const lp adM = mkp((lid() & 15) * 48 + STATM);
    {
      constexpr int PF = 4;
      bf16x8 qf[PF + 1];
      auto rowmax = [&](auto PR, auto Q) {
        f32x4 a = sa[2 * PR.v][Q.v], bb = sa[2 * PR.v + 1][Q.v];
        float m = max3(max3(a[0], a[1], a[2]), max3(a[3], bb[0], bb[1]), bb[2]);
        if constexpr (PR.v == 0) mx[Q.v] = max3(m, bb[3], bb[3]);
        else lds_max<Q.v * 4>(adM, max3(m, bb[3], mx[Q.v]));
      };
      sfor<0, PF>([&](auto N) { qf[N.v] = lds128(adL + N.v * 1024); });
      __builtin_amdgcn_sched_barrier(0);
      sfor<0, 108>([&](auto N) {
        constexpr int n = N.v, pr = n / 54, q = (n % 54) / 6, s = n % 6;
        if constexpr (n == 54) {
          STAMP(13 + u * 28);
          wait_vm<5>();
          STAMP(14 + u * 28);
          sfor<0, 6>([&](auto S) { pin(kf[2][S.v]); pin(kf[3][S.v]); });
          __builtin_amdgcn_sched_barrier(0);
        }
        if constexpr (n == 54 || n == 56 || n == 58) {
          kfix(kf[2][s], kf[2][s + 1]); kfix(kf[3][s], kf[3][s + 1]);
          __builtin_amdgcn_sched_barrier(0);
        }
        if constexpr (s == 0) { sa[2 * pr][q] = f32x4{0, 0, 0, 0}; sa[2 * pr + 1][q] = f32x4{0, 0, 0, 0}; }
        sa[2 * pr][q] = mma(kf[2 * pr][s], qf[n % (PF + 1)], sa[2 * pr][q]);
        __builtin_amdgcn_sched_barrier(0);
        if constexpr (n + PF < 108) qf[(n + PF) % (PF + 1)] = lds128(adL + ((n + PF) % 54) * 1024);
        __builtin_amdgcn_sched_barrier(0);
        sa[2 * pr + 1][q] = mma(kf[2 * pr + 1][s], qf[n % (PF + 1)], sa[2 * pr + 1][q]);
        __builtin_amdgcn_sched_barrier(0);
        if constexpr (n < 20 && n % 6 == 2) { dK0(kb, nk2, IC<21 + n / 6>{}); __builtin_amdgcn_sched_barrier(0); }
        if constexpr (n >= 20 && n < 104 && (n - 20) % 7 == 0) { dV(vb, (n - 20) / 21, IC<((n - 20) / 7) % 3>{}); __builtin_amdgcn_sched_barrier(0); }
        if constexpr (n == 103) { if (u == 0) touchW(wb0 + 4 * DH * 2); __builtin_amdgcn_sched_barrier(0); }
        if constexpr (s == 2 && n >= 6) { constexpr int pn = n - 6; rowmax(IC<pn / 54>{}, IC<(pn % 54) / 6>{}); __builtin_amdgcn_sched_barrier(0); }
      });
      asm volatile("s_nop 7\n\ts_nop 7\n\ts_nop 7");
      __builtin_amdgcn_sched_barrier(0);
      rowmax(IC<1>{}, IC<8>{});
    }
    STAMP(15 + u * 28);
    bar();
    STAMP(16 + u * 28);
    float sm[9];
    {
      f32x4 m0 = *(LDSP(f32x4))(adM), m1 = *(LDSP(f32x4))(adM + 16);
      float m2 = *(LDSP(float))(adM + 32);
      sfor<0, 4>([&](auto Q) { mx[Q.v] = -CS * m0[Q.v]; mx[4 + Q.v] = -CS * m1[Q.v]; });
      mx[8] = -CS * m2;
    }
    const lp adP = mkp(mkH() + (((w & 3) >> 1) * 9) * 1024 + (w & 1) * 512);
    auto exph = [&](auto J, auto K) {
      constexpr int j = J.v, pb = ((2 * j + HALF) % 3) * XBUF, q = K.v / 2;
      if constexpr (K.v >= 18) {
        constexpr int k = K.v - 18;
        float t;
        if constexpr (k < 2) t = bfly_sum(sm[4 * k], sm[4 * k + 1], sm[4 * k + 2], sm[4 * k + 3]);
        else t = bfly_sum(sm[8], sm[8], sm[8], sm[8]);
        unsigned l = lid();
        *(LDSP(float))(mkp(STATS + (l & 15) * 32 + (k < 2 ? (l >> 4) * 512 : 0) + w * 4) + k * 2048) = t;
      } else if constexpr ((K.v & 1) == 0) {
        f32x4 v = sa[j][q];
        f32x2 c2 = {CS, CS}, m2 = {mx[q], mx[q]};
        f32x2 lo = __builtin_elementwise_fma(f32x2{v[0], v[1]}, c2, m2), hi = __builtin_elementwise_fma(f32x2{v[2], v[3]}, c2, m2);
        sa[j][q] = f32x4{__builtin_amdgcn_exp2f(lo[0]), __builtin_amdgcn_exp2f(lo[1]), __builtin_amdgcn_exp2f(hi[0]), __builtin_amdgcn_exp2f(hi[1])};
      } else {
        f32x4 e = sa[j][q];
        f32x2 t = f32x2{e[0], e[1]} + f32x2{e[2], e[3]};
        if constexpr (j == 0) sm[q] = t[0] + t[1]; else sm[q] = (sm[q] + t[0]) + t[1];
        pinf(sm[q]);
        *(LDSP(u32x2))(adP + pb + q * 1024) = u32x2{pk2(e[0], e[1]), pk2(e[2], e[3])};
      }
    };
    sfor<0, 18>([&](auto K) { exph(IC<0>{}, K); });
    __builtin_amdgcn_sched_barrier(0);
    mkT();
    STAMP(17 + u * 28);
    wait_vm<9>();
    bar();
    STAMP(18 + u * 28);
    {
      const char* wn = wb0 + 4 * DH * 2; const char* xn = xb;
      sfor<0, 3>([&](auto T) { sfor<0, NQ>([&](auto Q) { acc[T.v][Q.v] = f32x4{0, 0, 0, 0}; }); });
      auto rd1 = [&](auto S, auto KS, auto R) {
        if constexpr (R.v == 0) fb[KS.v][0] = lds128(adL + (S.v % 3) * XBUF + (KS.v * 9 + QT0) * 1024);
        else if constexpr (R.v <= 3) rdT(S, KS, IC<R.v - 1>{});
        else fb[KS.v][R.v - 3] = lds128(adL + (S.v % 3) * XBUF + (KS.v * 9 + QT0 + R.v - 3) * 1024);
      };
      const char* kn = kb0 + 4 * SK * DH * 2;
      auto vm = [&](auto J, auto N) {
        constexpr int j = J.v, n = N.v;
        if constexpr (j <= 3) dV(vb, j + 4, N);
        else if constexpr (n < 3) dKn(kn, u, IC<3 * (j - 4) + n>{});
        else if (u == 0) { if constexpr (n < 6) dW(wn, j - 4, IC<n - 3>{}); else dX(xn, j - 5, IC<n - 6>{}); }
        else if constexpr (j == 4 && n < 5)
          dma4(((blockIdx.x >> 3) * 16 + w * 2 + (n - 3)) * 3840 + (lid() < 60 ? lid() : 59) * 64,
               (br ? p.wo[1] : p.wo[0]) + (size_t)(blockIdx.x & 3) * 1536 * 1280, ldsT);
      };
      float rs[NQ];
      f32x4 s0[NQ], s1[NQ];
      auto rsr = [&](auto Q) {
        const lp adS = mkp((lid() & 15) * 32 + STATS + QT0 * 512);
        s0[Q.v] = *(LDSP(f32x4))(adS + Q.v * 512); s1[Q.v] = *(LDSP(f32x4))(adS + Q.v * 512 + 16);
      };
      auto rsc = [&](auto Q) {
        f32x4 t = s0[Q.v] + s1[Q.v];
        rs[Q.v] = __builtin_amdgcn_rcpf((t[0] + t[1]) + (t[2] + t[3]));
        pinf(rs[Q.v]);
      };
      auto pvseg = [&](auto SG, auto NRD, auto&& rd, auto NV, auto&& ex) {
        constexpr int t = SG.v + 5 - 2 * HALF, j = t / 4, pt = t % 4;
        constexpr bool on = t >= 4 && t < 16;
        constexpr int e0 = pt == 0 ? 0 : pt == 1 ? 5 : pt == 2 ? 9 : 14, e1 = pt == 0 ? 5 : pt == 1 ? 9 : pt == 2 ? 14 : (j == 3 ? 21 : 18);
        constexpr int ne = on ? e1 - e0 : SG.v == 15 ? NQ : 0;
        auto eb = [&](auto K) { if constexpr (on) exph(IC<j>{}, IC<e0 + K.v>{}); else rsc(K); };
        if constexpr (SG.v < 0) sfor<0, ne>([&](auto K) { eb(K); __builtin_amdgcn_sched_barrier(0); });
        else seg(IC<(SG.v & 1)>{}, NRD, rd, IC<ne>{}, eb, NV, ex);
      };
      sfor<0, NR>([&](auto R) { rd1(IC<0>{}, IC<0>{}, R); });
      __builtin_amdgcn_sched_barrier(0);
      if constexpr (HALF == 0) pvseg(IC<-1>{}, IC<0>{}, nop, IC<0>{}, nop);
      sfor<0, 8>([&](auto S) {
        constexpr int i = S.v;
        constexpr int pv = i == 0 ? 0 : i <= 4 ? 3 : i == 5 ? 6 : 9, p1 = (pv + 1) / 2;
        pvseg(IC<2 * i>{}, IC<NR>{}, [&](auto R) { rd1(S, IC<1>{}, R); }, IC<pv - p1 + (i == 7 ? NQ : 0)>{},
              [&](auto K) { if constexpr (K.v < pv - p1) vm(IC<i - 1>{}, IC<p1 + K.v>{}); else rsr(IC<K.v - (pv - p1)>{}); });
        if constexpr (i <= 4) wait_vm<6>();
        else if constexpr (i == 5) { if (u == 0) wait_vm<9>(); else wait_vm<5>(); }
        else if constexpr (i == 6) { if (u == 0) wait_vm<15>(); else wait_vm<2>(); }
        else { if (u == 0) wait_vm<9>(); }
        bar();
        STAMP(19 + i + u * 28);
        if constexpr (i == 4) { mkK(); mkWV(offW, HD * 2); }
        if constexpr (i == 5) mkX();
        constexpr int cv = i <= 3 ? 3 : i == 4 ? 6 : 9;
        pvseg(IC<2 * i + 1>{}, IC<(i < 7 ? NR : 0)>{}, [&](auto R) { rd1(IC<i + 1>{}, IC<0>{}, R); }, IC<(i < 7 ? (cv + 1) / 2 : cv)>{}, [&](auto K) { vm(S, K); });
      });
      sfor<0, 6>([&](auto S) { undef8(kf[2][S.v]); });
      char* ob = p.ofr + ((size_t)((br * 8 + b) * 192 + (h0 + 4 * u) * 6) * 9 + QT0) * 1024 + (lid() << 4);
      const int sf = ut[0] >> 1, sh = ut[2] >> 1, hp = ut[2] & 1;
      sfor<0, NQ>([&](auto Q) {
        float r = rs[Q.v];
        f32x4 a0 = acc[0][Q.v] * r, a1 = acc[1][Q.v] * r, a2 = acc[2][Q.v] * r;
        *(u32x4*)(ob + (sf * 9 + Q.v) * 1024) = u32x4{pk2(a0[0], a0[1]), pk2(a0[2], a0[3]), pk2(a1[0], a1[1]), pk2(a1[2], a1[3])};
        *(u32x2*)(ob + (sh * 9 + Q.v) * 1024 + hp * 8) = u32x2{pk2(a2[0], a2[1]), pk2(a2[2], a2[3])};
      });
      __builtin_amdgcn_sched_barrier(0);
      STAMP(27 + u * 28);
    }
  }
  wait_vm<0>();
}

__global__ __launch_bounds__(512, 2) void k1_attn(P1 p) {
  extern __shared__ __attribute__((aligned(16))) char smem[];
  const int w = __builtin_amdgcn_readfirstlane(threadIdx.x >> 6), lane = threadIdx.x & 63;
  const int xcd = blockIdx.x & 7, idx = blockIdx.x >> 3;
  const int br = xcd >> 2, h0 = 8 * (xcd & 3) + (idx & 3), b = idx >> 2;
  if (threadIdx.x == 0 && blockIdx.x < 64) p.cnt[blockIdx.x * 32] = 0u;
  if (w < 4) k1_body<5, 0, 0>(p, w, lane, br, h0, b);
  else k1_body<4, 5, 1>(p, w, lane, br, h0, b);
}

constexpr int OBUF = 38912, OWO = 18432, LDS2 = 4 * OBUF;
struct P2 {
  const char* ofr; const char* wo[2]; char* slab; unsigned* cnt; char* out;
};
template <int NQ, int QT0, int NN>
__device__ __forceinline__ void k2_body(const P2& p, int w, int lane, int n0, int br, int b, int nt, int ksp) {
  const int g = lane >> 4, c = lane & 15;
  unsigned offW[3];
  sfor<0, 3>([&](auto I) {
    int o = 1024 * I.v + 16 * lane, rr = o / 320, ch = (o % 320) >> 4;
    offW[I.v] = (8 * w + rr) * 1280 + (((((ch >> 1) ^ ((rr >> 2) & 1)) << 1) | (ch & 1)) << 4);
  });
  const unsigned offO = 2304 * w + 16 * lane;
  const char* ob = p.ofr + (size_t)((br * 8 + b) * 192 + ksp * 48) * 9216;
  const char* wb = (br ? p.wo[1] : p.wo[0]) + (size_t)ksp * 1536 * 1280 + nt * 320;
  auto vm = [&](int i, auto BUF, auto N) {
    constexpr int n = N.v;
    if constexpr (n < 3) {
      unsigned l = BUF.v * OBUF + 2304 * w + n * 1024;
      if constexpr (n < 2) dma16(offO, ob + i * OWO + n * 1024, l);
      else if (lane < 16) dma16(offO, ob + i * OWO + n * 1024, l);
    } else {
      unsigned l = BUF.v * OBUF + OWO + 2560 * w + (n - 3) * 1024;
      if constexpr (n < 5) dma16(offW[n - 3], wb + i * 64 * 1280, l);
      else if (lane < 32) dma16(offW[2], wb + i * 64 * 1280, l);
    }
  };
  const unsigned rowp = OWO + (4 * g + (c >> 2)) * 320 + (c & 3) * 8 + n0 * 32;
  const unsigned aT0 = rowp + ((n0 & 1) ? -(g & 1) * 32 : (g & 1) * 32), aT1 = rowp + ((n0 & 1) ? (g & 1) * 32 : -(g & 1) * 32);
  const unsigned aO = QT0 * 1024 + lane * 16;
  constexpr int NM = NN * NQ, NR = NN + NQ;
  bf16x8 fa[2][NN], fb[2][NQ];
  f32x4 acc[NM];
  sfor<0, NM>([&](auto M) { acc[M.v] = f32x4{0, 0, 0, 0}; });
  lp pO, pT0, pT1;
  auto setp = [&](int bufi) { pO = mkp(aO + bufi * OBUF); pT0 = mkp(aT0 + bufi * OBUF); pT1 = mkp(aT1 + bufi * OBUF); };
  auto rd1 = [&](auto KS, auto R) {
    constexpr int ks = KS.v, r = R.v;
    if constexpr (r == 0) fb[ks][0] = lds128(pO + ks * 9216);
    else if constexpr (r <= NN) fa[ks][r - 1] = ldstr((((r - 1) & 1) ? pT1 : pT0) + (r - 1) * 32 + ks * 10240, 5120);
    else fb[ks][r - NN] = lds128(pO + ks * 9216 + (r - NN) * 1024);
  };
  auto seg = [&](auto KS, auto NI, auto&& item) {
    sfor<0, NM>([&](auto M) {
      acc[M.v] = mma(fa[KS.v][M.v % NN], fb[KS.v][M.v / NN], acc[M.v]);
      __builtin_amdgcn_sched_barrier(0);
      constexpr int per = (NI.v + NM - 1) / NM, lo = M.v * per < NI.v ? M.v * per : NI.v, hi = lo + per < NI.v ? lo + per : NI.v;
      sfor<lo, hi>([&](auto K) { item(K); __builtin_amdgcn_sched_barrier(0); });
    });
  };
  unsigned stv = 0;
  STAMP(0);
  sfor<0, 4>([&](auto S) { sfor<0, 6>([&](auto N) { vm(S.v, S, N); }); });
  wait_vm<18>(); bar();
  setp(0);
  sfor<0, NR>([&](auto R) { rd1(IC<0>{}, R); });
  __builtin_amdgcn_sched_barrier(0);
  auto stage = [&](int i, auto J, auto LAST) {
    constexpr int j = J.v;
    constexpr int pw = (LAST.v && j > 0) ? 0 : 3;
    seg(IC<0>{}, IC<NR + pw>{}, [&](auto K) {
      if constexpr (K.v < NR) rd1(IC<1>{}, K);
      else if (i > 0) vm(i + 3, IC<(j + 3) & 3>{}, IC<3 + K.v - NR>{});
    });
    if constexpr (!LAST.v || j == 0) wait_vm<12>(); else if constexpr (j == 1) wait_vm<6>(); else if constexpr (j == 2) wait_vm<0>();
    bar();
    STAMP(1 + i);
    constexpr bool fin = LAST.v && j == 3;
    if constexpr (!fin) setp((j + 1) & 3);
    seg(IC<1>{}, IC<(fin ? 0 : NR) + (LAST.v ? 0 : 3)>{}, [&](auto K) {
      if constexpr (K.v < NR) rd1(IC<0>{}, K); else vm(i + 4, J, IC<K.v - NR>{});
    });
  };
#pragma unroll 1
  for (int it = 0; it < 5; ++it) sfor<0, 4>([&](auto J) { stage(4 * it + J.v, J, IC<0>{}); });
  sfor<0, 4>([&](auto J) { stage(20 + J.v, J, IC<1>{}); });
  STAMP(25);
  const int grp = (br * 8 + b) * 4 + nt;
  char* sl = p.slab + ((size_t)(grp * 4 * 8 + w) * 16) * 1024 + lane * 16;
  f32x4 r[4][3];
  auto tail = [&](auto KSP) {
    constexpr int ks = KSP.v;
    sfor<0, NM>([&](auto M) { if constexpr ((M.v & 3) != ks) st_sc1(sl + (ks * 128 + M.v) * 1024, acc[M.v]); });
    STAMP(26);
    wait_vm<0>();
    bar();
    STAMP(27);
    if (w == 0) {
      if (lane == 0) {
        __hip_atomic_fetch_add(p.cnt + grp * 32, 1u, __ATOMIC_RELAXED, __HIP_MEMORY_SCOPE_AGENT);
        while (__hip_atomic_load(p.cnt + grp * 32, __ATOMIC_RELAXED, __HIP_MEMORY_SCOPE_AGENT) < 4u) __builtin_amdgcn_s_sleep(1);
      }
    }
    bar();
    STAMP(28);
    sfor<0, NM>([&](auto M) {
      if constexpr ((M.v & 3) == ks) sfor<0, 3>([&](auto O) { ld_sc1(r[M.v / 4][O.v], sl + ((O.v < ks ? O.v : O.v + 1) * 128 + M.v) * 1024); });
    });
    __builtin_amdgcn_sched_barrier(0);
    wait_vm<0>();
    sfor<0, NM>([&](auto M) {
      if constexpr ((M.v & 3) == ks) {
        sfor<0, 3>([&](auto O) { pin4(r[M.v / 4][O.v]); });
        f32x4 v[4];
        sfor<0, 4>([&](auto K) { if constexpr (K.v == ks) v[K.v] = acc[M.v]; else v[K.v] = r[M.v / 4][K.v < ks ? K.v : K.v - 1]; });
        f32x4 o = (v[0] + v[1]) + (v[2] + v[3]);
        *(u32x2*)(p.out + ((size_t)(b * 144 + 16 * (QT0 + M.v / NN) + c) * 1280 + br * 640 + nt * 160 + 16 * (n0 + M.v % NN) + 4 * g) * 2) =
            u32x2{pk2(o[0], o[1]), pk2(o[2], o[3])};
      }
    });
  };
  if (ksp == 0) tail(IC<0>{}); else if (ksp == 1) tail(IC<1>{}); else if (ksp == 2) tail(IC<2>{}); else tail(IC<3>{});
  STAMP(29);
}
__global__ __launch_bounds__(512, 2) void k2_oproj(P2 p) {
  extern __shared__ __attribute__((aligned(16))) char smem[];
  const int w = __builtin_amdgcn_readfirstlane(threadIdx.x >> 6), lane = threadIdx.x & 63;
  const int xcd = blockIdx.x & 7, idx = blockIdx.x >> 3;
  const int br = xcd >> 2, b = idx >> 2, nt = idx & 3, ksp = xcd & 3;
  if (w < 2) k2_body<5, 0, 3>(p, w, lane, 3 * w, br, b, nt, ksp);
  else if (w < 4) k2_body<5, 0, 2>(p, w, lane, 2 * w + 2, br, b, nt, ksp);
  else if (w < 6) k2_body<4, 5, 2>(p, w, lane, 2 * w - 8, br, b, nt, ksp);
  else k2_body<4, 5, 3>(p, w, lane, 3 * w - 14, br, b, nt, ksp);
}

extern "C" void kernel_launch(void* const* d_in, const int* in_sizes, int n_in, void* d_out, int out_size, void* d_ws,
                              size_t ws_size, hipStream_t stream) {
  P1 p;
  p.x[0] = (const char*)d_in[0]; p.k[0] = (const char*)d_in[1]; p.v[0] = (const char*)d_in[2]; p.wq[0] = (const char*)d_in[3];
  p.x[1] = (const char*)d_in[5]; p.k[1] = (const char*)d_in[6]; p.v[1] = (const char*)d_in[7]; p.wq[1] = (const char*)d_in[8];
  p.wo[0] = (const char*)d_in[4]; p.wo[1] = (const char*)d_in[9];
  p.ofr = (char*)d_ws;
  p.cnt = (unsigned*)((char*)d_ws + (30u << 20));
  P2 q;
  q.ofr = (const char*)d_ws; q.wo[0] = (const char*)d_in[4]; q.wo[1] = (const char*)d_in[9];
  q.slab = (char*)d_ws + (32u << 20); q.cnt = p.cnt; q.out = (char*)d_out;
  (void)hipFuncSetAttribute((const void*)k1_attn, hipFuncAttributeMaxDynamicSharedMemorySize, LDS1);
  (void)hipFuncSetAttribute((const void*)k2_oproj, hipFuncAttributeMaxDynamicSharedMemorySize, LDS2);
  k1_attn<<<256, 512, LDS1, stream>>>(p);
  k2_oproj<<<256, 512, LDS2, stream>>>(q);
}
```

```cpp
#pragma clang diagnostic ignored "-Wint-to-pointer-cast"
#include <hip/hip_runtime.h>
#include <hip/hip_bf16.h>
#include <stdint.h>

typedef __attribute__((ext_vector_type(8))) short bf16x8;
typedef __attribute__((ext_vector_type(4))) short bf16x4;
typedef __attribute__((ext_vector_type(4))) float f32x4;
typedef __attribute__((ext_vector_type(2))) float f32x2;
typedef __attribute__((ext_vector_type(2))) __bf16 bf2;
typedef __attribute__((ext_vector_type(2))) unsigned u32x2;
typedef __attribute__((ext_vector_type(4))) unsigned u32x4;
#define NTK " nt"
#define LDSP(T) __attribute__((address_space(3))) T*

constexpr int SQ = 144, DM = 640, NH = 32, SK = 512, DH = 192, HD = 6144;
constexpr int XBUF = 18432, SLOT = 24576, RING = 55296, STATM = 153600, STATS = 158208, LDS1 = 162816;

template <int I> struct IC { static constexpr int v = I; };
template <int B, int E, class F> __device__ __forceinline__ void sfor(F&& f) {
  if constexpr (B < E) { f(IC<B>{}); sfor<B + 1, E>(f); }
}
template <int N> __device__ __forceinline__ void wait_vm() { asm volatile("s_waitcnt vmcnt(%0)" ::"i"(N) : "memory"); }
__device__ __forceinline__ void bar() {
  __builtin_amdgcn_sched_barrier(0);
  asm volatile("s_waitcnt lgkmcnt(0)\n\ts_barrier" ::: "memory");
  __builtin_amdgcn_sched_barrier(0);
}
__device__ __forceinline__ void dma16(unsigned voff, const void* base, unsigned lds) {
  asm volatile("s_mov_b32 m0, %1\n\ts_nop 0\n\tglobal_load_lds_dwordx4 %0, %2" ::"v"(voff), "s"(lds), "s"(base) : "memory");
}
__device__ __forceinline__ void dma16nt(unsigned voff, const void* base, unsigned lds) {
  asm volatile("s_mov_b32 m0, %1\n\ts_nop 0\n\tglobal_load_lds_dwordx4 %0, %2" NTK ::"v"(voff), "s"(lds), "s"(base) : "memory");
}
template <int OFF> __device__ __forceinline__ void gload_nt_if(bf16x8& d, unsigned voff, const void* base, int sk) {
  asm volatile("s_cmp_lg_u32 %4, 0\n\ts_cbranch_scc1 .Lk%=\n\tglobal_load_dwordx4 %0, %1, %2 offset:%3" NTK "\n.Lk%=:"
               : "=v"(d) : "v"(voff), "s"(base), "i"(OFF), "s"(sk) : "memory", "scc");
}
template <int OFF> __device__ __forceinline__ void gload_nt_keep(bf16x8& d, unsigned voff, const void* base, int sk) {
  asm volatile("s_cmp_lg_u32 %4, 0\n\ts_cbranch_scc1 .Lk%=\n\tglobal_load_dwordx4 %0, %1, %2 offset:%3" NTK "\n.Lk%=:"
               : "+v"(d) : "v"(voff), "s"(base), "i"(OFF), "s"(sk) : "memory", "scc");
}
__device__ __forceinline__ void undef8(bf16x8& d) { asm volatile("" : "=v"(d)); }
__device__ __forceinline__ void dma4(unsigned voff, const void* base, unsigned lds) {
  asm volatile("s_mov_b32 m0, %1\n\ts_nop 0\n\tglobal_load_lds_dword %0, %2" ::"v"(voff), "s"(lds), "s"(base) : "memory");
}
__device__ __forceinline__ void dma4nt(unsigned voff, const void* base, unsigned lds) {
  asm volatile("s_mov_b32 m0, %1\n\ts_nop 0\n\tglobal_load_lds_dword %0, %2 nt" ::"v"(voff), "s"(lds), "s"(base) : "memory");
}
#define DMAT dma4
#define DMAW dma16
#define DMAO dma4
template <class T> __device__ __forceinline__ void ost(char* a, T v) {
  *(T*)a = v;
}
__device__ __forceinline__ void st2_sc1(char* a, u32x2 v) { asm volatile("global_store_dwordx2 %0, %1, off sc1\n\ts_nop 1" ::"v"(a), "v"(v) : "memory"); }
__device__ __forceinline__ void ld2_sc1(u32x2& d, const char* a) { asm volatile("global_load_dwordx2 %0, %1, off sc1" : "=v"(d) : "v"(a) : "memory"); }
__device__ __forceinline__ void pinu4(u32x4& d) { asm volatile("" : "+v"(d)); }
__device__ __forceinline__ void pinu2(u32x2& d) { asm volatile("" : "+v"(d)); }
__device__ __forceinline__ unsigned pkn(float a, float b) { return __builtin_bit_cast(unsigned, __builtin_amdgcn_cvt_pknorm_i16(a, b)); }
__device__ __forceinline__ unsigned umax(unsigned a, unsigned b) { return a > b ? a : b; }
__device__ __forceinline__ unsigned wave_umax(unsigned v) {
  v = umax(v, __builtin_amdgcn_update_dpp(v, v, 0x121, 0xF, 0xF, false));
  v = umax(v, __builtin_amdgcn_update_dpp(v, v, 0x122, 0xF, 0xF, false));
  v = umax(v, __builtin_amdgcn_update_dpp(v, v, 0x124, 0xF, 0xF, false));
  v = umax(v, __builtin_amdgcn_update_dpp(v, v, 0x128, 0xF, 0xF, false));
  auto r = __builtin_amdgcn_permlane16_swap(v, v, false, false);
  v = umax(r[0], r[1]);
  auto t = __builtin_amdgcn_permlane32_swap(v, v, false, false);
  return __builtin_amdgcn_readfirstlane(umax(t[0], t[1]));
}
__device__ __forceinline__ void st_sc1(char* a, u32x4 v) { asm volatile("global_store_dwordx4 %0, %1, off sc1\n\ts_nop 1" ::"v"(a), "v"(v) : "memory"); }
__device__ __forceinline__ void ld_sc1(u32x4& d, const char* a) { asm volatile("global_load_dwordx4 %0, %1, off sc1" : "=v"(d) : "v"(a) : "memory"); }
__device__ __forceinline__ void pin(bf16x8& d) { asm volatile("" : "+v"(d)); }
__device__ __forceinline__ void pinf(float& d) { asm volatile("" : "+v"(d)); }
__device__ __forceinline__ unsigned opq(unsigned d) { asm volatile("" : "+v"(d)); return d; }
__device__ __forceinline__ unsigned lid() {
  unsigned d; asm volatile("v_mbcnt_lo_u32_b32 %0, -1, 0\n\tv_mbcnt_hi_u32_b32 %0, -1, %0" : "=v"(d)); return d;
}
typedef LDSP(char) lp;
__device__ __forceinline__ lp mkp(unsigned a) { return (lp)opq(a); }
__device__ __forceinline__ bf16x8 lds128(lp a) { return *(LDSP(bf16x8))a; }
__device__ __forceinline__ bf16x8 ldstr(lp a, int d) {
  bf16x4 lo = __builtin_amdgcn_ds_read_tr16_b64_v4i16((LDSP(bf16x4))a);
  bf16x4 hi = __builtin_amdgcn_ds_read_tr16_b64_v4i16((LDSP(bf16x4))(a + d));
  return __builtin_shufflevector(lo, hi, 0, 1, 2, 3, 4, 5, 6, 7);
}
__device__ __forceinline__ unsigned pk2(float a, float b) {
  f32x2 v = {a, b};
  return __builtin_bit_cast(unsigned, __builtin_convertvector(v, bf2));
}
__device__ __forceinline__ f32x4 mma(bf16x8 a, bf16x8 b, f32x4 c) {
  typedef __attribute__((ext_vector_type(8))) __bf16 b8;
  return __builtin_amdgcn_mfma_f32_16x16x32_bf16(__builtin_bit_cast(b8, a), __builtin_bit_cast(b8, b), c, 0, 0, 0);
}
__device__ __forceinline__ void kfix(bf16x8& a, bf16x8& b) {
  u32x4 x = __builtin_bit_cast(u32x4, a), y = __builtin_bit_cast(u32x4, b), p, q;
  for (int i = 0; i < 4; ++i) {
    p[i] = __builtin_amdgcn_update_dpp(x[i], y[i], 0x128, 0xF, 0xC, false);
    q[i] = __builtin_amdgcn_update_dpp(y[i], x[i], 0x128, 0xF, 0x3, false);
  }
  a = __builtin_bit_cast(bf16x8, p); b = __builtin_bit_cast(bf16x8, q);
}
__device__ __forceinline__ float max3(float a, float b, float c) { float d; asm("v_max3_f32 %0, %1, %2, %3" : "=v"(d) : "v"(a), "v"(b), "v"(c)); return d; }
template <int OFF> __device__ __forceinline__ void lds_max(lp a, float v) { asm volatile("ds_max_f32 %0, %1 offset:%2" ::"v"(a), "v"(v), "i"(OFF) : "memory"); }
__device__ __forceinline__ float bfly_sum(float a, float b, float c, float d) {
  auto r = __builtin_amdgcn_permlane16_swap(__float_as_uint(a), __float_as_uint(b), false, false);
  auto s = __builtin_amdgcn_permlane16_swap(__float_as_uint(c), __float_as_uint(d), false, false);
  float ab = __uint_as_float(r[0]) + __uint_as_float(r[1]), cd = __uint_as_float(s[0]) + __uint_as_float(s[1]);
  auto t = __builtin_amdgcn_permlane32_swap(__float_as_uint(ab), __float_as_uint(cd), false, false);
  return __uint_as_float(t[0]) + __uint_as_float(t[1]);
}
#define STAMP(k)
struct P1 {
  const char* x[2]; const char* k[2]; const char* v[2]; const char* wq[2];
  const char* wo[2];
  unsigned* cnt;
  char* ofr;
};

#define EARLYX 0
template <int NQ, int QT0, int HALF>
__device__ __forceinline__ void k1_body(const P1& p, int w, int lane, int br, int h0, int b) {
  const int dq = w & 3;
  unsigned offW[3], offV[3], offX[3], offK;
  auto mkWV = [&](unsigned* off, int rs) {
    const unsigned l = lid();
    sfor<0, 3>([&](auto I) {
      unsigned t = 64 * I.v + l, rr = (t * 2731u) >> 16, ch = t - 24 * rr;
      unsigned hh = ((rr >> 1) & 1) | ((w & 1) << 1);
      off[I.v] = (8 * w + rr) * rs + ((ch ^ (hh << 1)) << 4);
    });
  };
  auto mkX = [&]() {
    const unsigned l = lid();
    sfor<0, 3>([&](auto I) {
      unsigned row = 18 * w + 8 * I.v + (l >> 3);
      offX[I.v] = row * (DM * 2) + (((l & 7) ^ ((row >> 1) & 7)) << 4);
    });
  };
  auto mkK = [&]() { const unsigned l = lid(); offK = (16 * w + (l & 7)) * 384 + (l & 8) * 8 + (l & 48); };
  const unsigned ldsW = RING + w * 3072, ldsX = w * 2304;
  lp adT[3];
  int ut[3];
  sfor<0, 3>([&](auto I) { ut[I.v] = 3 * dq + ((dq & 1) ? (I.v + 1) % 3 : I.v); });
  auto mkT = [&]() {
    const unsigned l = lid(), q4 = (l >> 2) & 3, hh = (q4 >> 1) | ((l >> 3) & 2);
    sfor<0, 3>([&](auto I) { adT[I.v] = mkp(RING + (8 * (l >> 4) + q4) * 384 + ((ut[I.v] ^ hh) << 5) + ((l & 3) << 3)); });
  };
  lp adX[2];
  auto mkXr = [&]() {
    const unsigned l = lid(), cc = l & 15, gg = l >> 4;
    adX[0] = mkp((QT0 * 16 + cc) * 128 + ((gg ^ (cc >> 1)) << 4));
    adX[1] = mkp((QT0 * 16 + cc) * 128 + (((4 + gg) ^ (cc >> 1)) << 4));
  };
  auto mkH = [&]() { const unsigned l = lid(); return ((l >> 5) * 16 + (l & 15)) * 16 + ((l >> 1) & 8); };
  mkWV(offW, HD * 2); mkX(); mkK(); mkT();
  const lp adL = mkp(lane * 16);
  const float CS = 0.07216878364870322f * 1.4426950408889634f;

  auto dW = [&](const char* wb, int i, auto I) {
    DMAW(offW[I.v], wb + (size_t)i * 64 * HD * 2, ldsW + (i & 3) * SLOT + I.v * 1024); };
  auto dX = [&](const char* xb, int i, auto I) {
    unsigned l = ldsX + ((i + 2) % 3) * XBUF + I.v * 1024;
    if constexpr (I.v < 2) DMAW(offX[I.v], xb + i * 128, l);
    else if (lid() < 16) DMAW(offX[2], xb + i * 128, l);
  };
  auto dV = [&](const char* vb, int i, auto I) {
    dma16nt(offV[I.v], vb + i * SLOT, ldsW + (i & 3) * SLOT + I.v * 1024); };
  auto issueW = [&](const char* wb, int i) { sfor<0, 3>([&](auto I) { dW(wb, i, I); }); };
  auto issueX = [&](const char* xb, int i) { sfor<0, 3>([&](auto I) { dX(xb, i, I); }); };
  constexpr int NST = 2 * NQ, NM = 3 * NQ, NR = 3 + NQ;
  unsigned stv = 0;
  STAMP(0);

  const char* xb = (br ? p.x[1] : p.x[0]) + (size_t)b * SQ * DM * 2;
  const char* wb0 = (br ? p.wq[1] : p.wq[0]) + (size_t)h0 * DH * 2;
  const size_t kvo = (size_t)(b * NH + h0) * SK * DH * 2;
  const char* kb0 = (br ? p.k[1] : p.k[0]) + kvo;
  const char* vb0 = (br ? p.v[1] : p.v[0]) + kvo;
  bf16x8 kf[4][6];
  auto dKn = [&](const char* kb, int sk, auto M) {
    constexpr int m = M.v, L = m / 4, jt = (m >> 1) & 1, hf = m & 1;
    gload_nt_if<L * 128 + hf * 3072>(kf[jt][2 * L + hf], offK, kb + jt * 49152, sk);
  };
  auto dK0 = [&](const char* kb, int sk, auto M) {
    constexpr int m = M.v, L = m < 12 ? m / 4 : (m % 6) / 2, jt = m < 12 ? (m >> 1) & 1 : m / 6, hf = m & 1;
    if constexpr (m < 18) gload_nt_keep<L * 128 + hf * 3072>(kf[jt][2 * L + hf], offK, kb + jt * 49152, sk);
    else gload_nt_if<L * 128 + hf * 3072>(kf[jt][2 * L + hf], offK, kb + jt * 49152, 0);
  };
  const unsigned ldsT = STATM + 1024 + w * 256;
  auto touchW = [&](const char* wb) {
    unsigned l = lid(); l = l < 60 ? l : 59;
    unsigned r = (l * 43u) >> 8;
    DMAT((80 * b + 10 * w + r) * (HD * 2) + (l - 6 * r) * 64, wb, ldsT);
  };
  const int late = (b & 1) ^ EARLYX;
  touchW(wb0);
  { unsigned l = lid(); l = l < 45 ? l : 44;
    DMAT((h0 & 3) * 46080 + (90 * w + l) * 64, xb, ldsT); DMAT((h0 & 3) * 46080 + (90 * w + 45 + l) * 64, xb, ldsT); }
  sfor<0, 6>([&](auto S) { undef8(kf[0][S.v]); undef8(kf[1][S.v]); undef8(kf[2][S.v]); });
  sfor<0, 18>([&](auto M) { dK0(kb0, late, M); });
  issueW(wb0, 0); issueX(xb, 0); issueW(wb0, 1); issueX(xb, 1);
  wait_vm<6>(); bar();
#pragma unroll 1
  for (int u = 0; u < 2; ++u) {
    const char* wb = wb0 + u * (4 * DH * 2);
    const char* kb = kb0 + u * (4 * SK * DH * 2);
    const char* vb = vb0 + u * (4 * SK * DH * 2);
    STAMP(1 + u * 28);
    const int nk0 = u | (late ^ 1), nk1 = (u & late) ^ 1, nk2 = u ^ late ^ 1;
    bf16x8 fa[2][3], fb[2][NQ];
    f32x4 acc[3][NQ];
    auto rdT = [&](auto S, auto KS, auto T) {
      constexpr int i = S.v, ks = KS.v;
      if constexpr ((i & 3) == 3) fa[ks][T.v] = ldstr(mkp((unsigned)(size_t)adT[T.v] + 3 * SLOT) + ks * 12288, 1536);
      else fa[ks][T.v] = ldstr(adT[T.v] + (i & 3) * SLOT + ks * 12288, 1536);
    };
    auto seg = [&](auto KS, auto NA, auto&& ia, auto NB, auto&& ib, auto NC, auto&& ic) {
      sfor<0, NM>([&](auto M) {
        constexpr int m = M.v, q = m / 3, t = m % 3;
        acc[t][q] = mma(fa[KS.v][t], fb[KS.v][q], acc[t][q]);
        __builtin_amdgcn_sched_barrier(0);
        if constexpr (m < NA.v) { ia(M); __builtin_amdgcn_sched_barrier(0); }
        sfor<(m * NB.v + NM - 1) / NM, ((m + 1) * NB.v + NM - 1) / NM>([&](auto K) { ib(K); __builtin_amdgcn_sched_barrier(0); });
        sfor<(m * NC.v + NM / 2) / NM, ((m + 1) * NC.v + NM / 2) / NM>([&](auto K) { ic(K); __builtin_amdgcn_sched_barrier(0); });
      });
    };
    auto nop = [&](auto) {};
    {
      sfor<0, 3>([&](auto T) { sfor<0, NQ>([&](auto Q) { acc[T.v][Q.v] = f32x4{0, 0, 0, 0}; }); });
      auto rd1 = [&](auto S, auto KS, auto R) {
        if constexpr (R.v == 0) fb[KS.v][0] = lds128(adX[KS.v] + ((S.v + 2) % 3) * XBUF);
        else if constexpr (R.v <= 3) rdT(S, KS, IC<R.v - 1>{});
        else fb[KS.v][R.v - 3] = lds128(adX[KS.v] + ((S.v + 2) % 3) * XBUF + (R.v - 3) * 2048);
      };
      auto vm = [&](auto J, auto N) {
        constexpr int j = J.v, n = N.v;
        if constexpr (j < 0) { if (u == 0) { if constexpr (n < 3) dW(wb, 2, N); else if constexpr (n < 6) dX(xb, 2, IC<n - 3>{}); else dW(wb, 3, IC<n - 6>{}); } }
        else if constexpr (j <= 5) { if constexpr (n < 3) dX(xb, j + 3, N); else if constexpr (n < 6) dW(wb, j + 4, IC<n - 3>{}); else dK0(kb, n < 18 ? nk0 : nk1, IC<n - 6>{}); }
        else if constexpr (j == 6) { if constexpr (n < 3) dX(xb, 9, N); else dK0(kb, nk2, IC<12 + n - 3>{}); }
        else if constexpr (j == 7) dK0(kb, nk2, IC<16 + n>{});
        else dK0(kb, nk2, IC<18 + n>{});
      };
      mkXr();
      sfor<0, NR>([&](auto R) { rd1(IC<0>{}, IC<0>{}, R); });
      __builtin_amdgcn_sched_barrier(0);
      sfor<0, 10>([&](auto S) {
        constexpr int i = S.v;
        constexpr int pv = i == 0 ? 9 : i == 4 ? 24 : i <= 6 ? 6 : i == 7 ? 7 : i == 8 ? 2 : 3, p1 = i == 0 ? 0 : (pv + 1) / 2;
        seg(IC<0>{}, IC<NR>{}, [&](auto R) { rd1(S, IC<1>{}, R); }, IC<pv - p1>{}, [&](auto K) { vm(IC<i - 1>{}, IC<p1 + K.v>{}); },
            IC<(i == 9 ? 3 : 0)>{}, [&](auto L) { kfix(kf[1][2 * L.v], kf[1][2 * L.v + 1]); pin(kf[1][2 * L.v]); pin(kf[1][2 * L.v + 1]); });
        if constexpr (i == 0) { if (u == 0) wait_vm<9>(); else wait_vm<9 + NST>(); }
        else if constexpr (i == 1) { if (u == 0) wait_vm<9>(); else wait_vm<6 + NST>(); }
        else if constexpr (i == 4 || i == 5) { if (nk0 == 0) wait_vm<21>(); else if (nk1 == 0) wait_vm<15>(); else wait_vm<9>(); }
        else if constexpr (i <= 6) wait_vm<9>();
        else if constexpr (i == 7) { if (nk2 == 0) wait_vm<10>(); else wait_vm<6>(); }
        else if constexpr (i == 8) { if (nk2 == 0) wait_vm<6>(); else wait_vm<0>(); }
        bar();
        STAMP(2 + i + u * 28);
        if constexpr (i == 8) { sfor<0, 6>([&](auto S2) { pin(kf[0][S2.v]); pin(kf[1][S2.v]); }); __builtin_amdgcn_sched_barrier(0); }
        constexpr int cv = i == 3 ? 24 : i <= 5 ? 6 : i == 6 ? 7 : i == 7 ? 2 : i == 8 ? 3 : 0;
        seg(IC<1>{}, IC<(i < 9 ? NR : 0)>{}, [&](auto R) { rd1(IC<i + 1>{}, IC<0>{}, R); }, IC<(cv + 1) / 2>{}, [&](auto K) { vm(S, K); },
            IC<(i == 8 ? 3 : 0)>{}, [&](auto L) { kfix(kf[0][2 * L.v], kf[0][2 * L.v + 1]); pin(kf[0][2 * L.v]); pin(kf[0][2 * L.v + 1]); });
      });
      mkWV(offV, 384);
      if (w < 3) *(LDSP(float))(mkp(STATM + w * 256 + (lid() << 2))) = -__builtin_inff();
      const unsigned adH = mkH();
      sfor<0, 3>([&](auto T) {
        lp a = mkp(adH + (ut[T.v] >> 1) * 1024 + (ut[T.v] & 1) * 512) + QT0 * 6144;
        sfor<0, NQ>([&](auto Q) {
          f32x4 v = acc[T.v][Q.v];
          *(LDSP(u32x2))(a + Q.v * 6144) = u32x2{pk2(v[0], v[1]), pk2(v[2], v[3])};
        });
      });
      bar();
    }
    STAMP(12 + u * 28);
    f32x4 sa[4][9];
    float mx[9];
    const lp adM = mkp((lid() & 15) * 48 + STATM);
    {
      constexpr int PF = 4;
      bf16x8 qf[PF + 1];
      auto rowmax = [&](auto PR, auto Q) {
        f32x4 a = sa[2 * PR.v][Q.v], bb = sa[2 * PR.v + 1][Q.v];
        float m = max3(max3(a[0], a[1], a[2]), max3(a[3], bb[0], bb[1]), bb[2]);
        if constexpr (PR.v == 0) mx[Q.v] = max3(m, bb[3], bb[3]);
        else lds_max<Q.v * 4>(adM, max3(m, bb[3], mx[Q.v]));
      };
      sfor<0, PF>([&](auto N) { qf[N.v] = lds128(adL + N.v * 1024); });
      __builtin_amdgcn_sched_barrier(0);
      sfor<0, 108>([&](auto N) {
        constexpr int n = N.v, pr = n / 54, q = (n % 54) / 6, s = n % 6;
        if constexpr (n == 54) {
          STAMP(13 + u * 28);
          wait_vm<5>();
          STAMP(14 + u * 28);
          sfor<0, 6>([&](auto S) { pin(kf[2][S.v]); pin(kf[3][S.v]); });
          __builtin_amdgcn_sched_barrier(0);
        }
        if constexpr (n == 54 || n == 56 || n == 58) {
          kfix(kf[2][s], kf[2][s + 1]); kfix(kf[3][s], kf[3][s + 1]);
          __builtin_amdgcn_sched_barrier(0);
        }
        if constexpr (s == 0) { sa[2 * pr][q] = f32x4{0, 0, 0, 0}; sa[2 * pr + 1][q] = f32x4{0, 0, 0, 0}; }
        sa[2 * pr][q] = mma(kf[2 * pr][s], qf[n % (PF + 1)], sa[2 * pr][q]);
        __builtin_amdgcn_sched_barrier(0);
        if constexpr (n + PF < 108) qf[(n + PF) % (PF + 1)] = lds128(adL + ((n + PF) % 54) * 1024);
        __builtin_amdgcn_sched_barrier(0);
        sa[2 * pr + 1][q] = mma(kf[2 * pr + 1][s], qf[n % (PF + 1)], sa[2 * pr + 1][q]);
        __builtin_amdgcn_sched_barrier(0);
        if constexpr (n < 20 && n % 6 == 2) { dK0(kb, nk2, IC<21 + n / 6>{}); __builtin_amdgcn_sched_barrier(0); }
        if constexpr (n >= 20 && n < 104 && (n - 20) % 7 == 0) { dV(vb, (n - 20) / 21, IC<((n - 20) / 7) % 3>{}); __builtin_amdgcn_sched_barrier(0); }
        if constexpr (n == 103) { if (u == 0) touchW(wb0 + 4 * DH * 2); __builtin_amdgcn_sched_barrier(0); }
        if constexpr (s == 2 && n >= 6) { constexpr int pn = n - 6; rowmax(IC<pn / 54>{}, IC<(pn % 54) / 6>{}); __builtin_amdgcn_sched_barrier(0); }
      });
      asm volatile("s_nop 7\n\ts_nop 7\n\ts_nop 7");
      __builtin_amdgcn_sched_barrier(0);
      rowmax(IC<1>{}, IC<8>{});
    }
    STAMP(15 + u * 28);
    bar();
    STAMP(16 + u * 28);
    float sm[9];
    {
      f32x4 m0 = *(LDSP(f32x4))(adM), m1 = *(LDSP(f32x4))(adM + 16);
      float m2 = *(LDSP(float))(adM + 32);
      sfor<0, 4>([&](auto Q) { mx[Q.v] = -CS * m0[Q.v]; mx[4 + Q.v] = -CS * m1[Q.v]; });
      mx[8] = -CS * m2;
    }
    const lp adP = mkp(mkH() + (((w & 3) >> 1) * 9) * 1024 + (w & 1) * 512);
    auto exph = [&](auto J, auto K) {
      constexpr int j = J.v, pb = ((2 * j + HALF) % 3) * XBUF, q = K.v / 2;
      if constexpr (K.v >= 18) {
        constexpr int k = K.v - 18;
        float t;
        if constexpr (k < 2) t = bfly_sum(sm[4 * k], sm[4 * k + 1], sm[4 * k + 2], sm[4 * k + 3]);
        else t = bfly_sum(sm[8], sm[8], sm[8], sm[8]);
        unsigned l = lid();
        *(LDSP(float))(mkp(STATS + (l & 15) * 32 + (k < 2 ? (l >> 4) * 512 : 0) + w * 4) + k * 2048) = t;
      } else if constexpr ((K.v & 1) == 0) {
        f32x4 v = sa[j][q];
        f32x2 c2 = {CS, CS}, m2 = {mx[q], mx[q]};
        f32x2 lo = __builtin_elementwise_fma(f32x2{v[0], v[1]}, c2, m2), hi = __builtin_elementwise_fma(f32x2{v[2], v[3]}, c2, m2);
        sa[j][q] = f32x4{__builtin_amdgcn_exp2f(lo[0]), __builtin_amdgcn_exp2f(lo[1]), __builtin_amdgcn_exp2f(hi[0]), __builtin_amdgcn_exp2f(hi[1])};
      } else {
        f32x4 e = sa[j][q];
        f32x2 t = f32x2{e[0], e[1]} + f32x2{e[2], e[3]};
        if constexpr (j == 0) sm[q] = t[0] + t[1]; else sm[q] = (sm[q] + t[0]) + t[1];
        pinf(sm[q]);
        *(LDSP(u32x2))(adP + pb + q * 1024) = u32x2{pk2(e[0], e[1]), pk2(e[2], e[3])};
      }
    };
    sfor<0, 18>([&](auto K) { exph(IC<0>{}, K); });
    __builtin_amdgcn_sched_barrier(0);
    mkT();
    STAMP(17 + u * 28);
    wait_vm<9>();
    bar();
    STAMP(18 + u * 28);
    {
      const char* wn = wb0 + 4 * DH * 2; const char* xn = xb;
      sfor<0, 3>([&](auto T) { sfor<0, NQ>([&](auto Q) { acc[T.v][Q.v] = f32x4{0, 0, 0, 0}; }); });
      auto rd1 = [&](auto S, auto KS, auto R) {
        if constexpr (R.v == 0) fb[KS.v][0] = lds128(adL + (S.v % 3) * XBUF + (KS.v * 9 + QT0) * 1024);
        else if constexpr (R.v <= 3) rdT(S, KS, IC<R.v - 1>{});
        else fb[KS.v][R.v - 3] = lds128(adL + (S.v % 3) * XBUF + (KS.v * 9 + QT0 + R.v - 3) * 1024);
      };
      const char* kn = kb0 + 4 * SK * DH * 2;
      auto vm = [&](auto J, auto N) {
        constexpr int j = J.v, n = N.v;
        if constexpr (j <= 3) dV(vb, j + 4, N);
        else if constexpr (n < 3) dKn(kn, u, IC<3 * (j - 4) + n>{});
        else if (u == 0) { if constexpr (n < 6) dW(wn, j - 4, IC<n - 3>{}); else dX(xn, j - 5, IC<n - 6>{}); }
        else if constexpr (j == 4 && n < 5)
          DMAO(((blockIdx.x >> 3) * 16 + w * 2 + (n - 3)) * 3840 + (lid() < 60 ? lid() : 59) * 64,
               (br ? p.wo[1] : p.wo[0]) + (size_t)(blockIdx.x & 3) * 1536 * 1280, ldsT);
      };
      float rs[NQ];
      f32x4 s0[NQ], s1[NQ];
      auto rsr = [&](auto Q) {
        const lp adS = mkp((lid() & 15) * 32 + STATS + QT0 * 512);
        s0[Q.v] = *(LDSP(f32x4))(adS + Q.v * 512); s1[Q.v] = *(LDSP(f32x4))(adS + Q.v * 512 + 16);
      };
      auto rsc = [&](auto Q) {
        f32x4 t = s0[Q.v] + s1[Q.v];
        rs[Q.v] = __builtin_amdgcn_rcpf((t[0] + t[1]) + (t[2] + t[3]));
        pinf(rs[Q.v]);
      };
      auto pvseg = [&](auto SG, auto NRD, auto&& rd, auto NV, auto&& ex) {
        constexpr int t = SG.v + 5 - 2 * HALF, j = t / 4, pt = t % 4;
        constexpr bool on = t >= 4 && t < 16;
        constexpr int e0 = pt == 0 ? 0 : pt == 1 ? 5 : pt == 2 ? 9 : 14, e1 = pt == 0 ? 5 : pt == 1 ? 9 : pt == 2 ? 14 : (j == 3 ? 21 : 18);
        constexpr int ne = on ? e1 - e0 : SG.v == 15 ? NQ : 0;
        auto eb = [&](auto K) { if constexpr (on) exph(IC<j>{}, IC<e0 + K.v>{}); else rsc(K); };
        if constexpr (SG.v < 0) sfor<0, ne>([&](auto K) { eb(K); __builtin_amdgcn_sched_barrier(0); });
        else seg(IC<(SG.v & 1)>{}, NRD, rd, IC<ne>{}, eb, NV, ex);
      };
      sfor<0, NR>([&](auto R) { rd1(IC<0>{}, IC<0>{}, R); });
      __builtin_amdgcn_sched_barrier(0);
      if constexpr (HALF == 0) pvseg(IC<-1>{}, IC<0>{}, nop, IC<0>{}, nop);
      sfor<0, 8>([&](auto S) {
        constexpr int i = S.v;
        constexpr int pv = i == 0 ? 0 : i <= 4 ? 3 : i == 5 ? 6 : 9, p1 = (pv + 1) / 2;
        pvseg(IC<2 * i>{}, IC<NR>{}, [&](auto R) { rd1(S, IC<1>{}, R); }, IC<pv - p1 + (i == 7 ? NQ : 0)>{},
              [&](auto K) { if constexpr (K.v < pv - p1) vm(IC<i - 1>{}, IC<p1 + K.v>{}); else rsr(IC<K.v - (pv - p1)>{}); });
        if constexpr (i <= 4) wait_vm<6>();
        else if constexpr (i == 5) { if (u == 0) wait_vm<9>(); else wait_vm<5>(); }
        else if constexpr (i == 6) { if (u == 0) wait_vm<15>(); else wait_vm<2>(); }
        else { if (u == 0) wait_vm<9>(); }
        bar();
        STAMP(19 + i + u * 28);
        if constexpr (i == 4) { mkK(); mkWV(offW, HD * 2); }
        if constexpr (i == 5) mkX();
        constexpr int cv = i <= 3 ? 3 : i == 4 ? 6 : 9;
        pvseg(IC<2 * i + 1>{}, IC<(i < 7 ? NR : 0)>{}, [&](auto R) { rd1(IC<i + 1>{}, IC<0>{}, R); }, IC<(i < 7 ? (cv + 1) / 2 : cv)>{}, [&](auto K) { vm(S, K); });
      });
      sfor<0, 6>([&](auto S) { undef8(kf[2][S.v]); });
      char* ob = p.ofr + ((size_t)((br * 8 + b) * 192 + (h0 + 4 * u) * 6) * 9 + QT0) * 1024 + (lid() << 4);
      const int sf = ut[0] >> 1, sh = ut[2] >> 1, hp = ut[2] & 1;
      sfor<0, NQ>([&](auto Q) {
        float r = rs[Q.v];
        f32x4 a0 = acc[0][Q.v] * r, a1 = acc[1][Q.v] * r, a2 = acc[2][Q.v] * r;
        ost(ob + (sf * 9 + Q.v) * 1024, u32x4{pk2(a0[0], a0[1]), pk2(a0[2], a0[3]), pk2(a1[0], a1[1]), pk2(a1[2], a1[3])});
        ost(ob + (sh * 9 + Q.v) * 1024 + hp * 8, u32x2{pk2(a2[0], a2[1]), pk2(a2[2], a2[3])});
      });
      __builtin_amdgcn_sched_barrier(0);
      STAMP(27 + u * 28);
    }
  }
  wait_vm<0>();
}

__global__ __launch_bounds__(512, 2) void k1_attn(P1 p) {
  extern __shared__ __attribute__((aligned(16))) char smem[];
  const int w = __builtin_amdgcn_readfirstlane(threadIdx.x >> 6), lane = threadIdx.x & 63;
  const int xcd = blockIdx.x & 7, idx = blockIdx.x >> 3;
  const int br = xcd >> 2, h0 = 8 * (xcd & 3) + (idx & 3), b = idx >> 2;
  if (threadIdx.x < 8) p.cnt[(blockIdx.x * 2 + (threadIdx.x >> 2)) * 32 + (threadIdx.x & 3)] = 0u;
  if (w < 4) k1_body<5, 0, 0>(p, w, lane, br, h0, b);
  else k1_body<4, 5, 1>(p, w, lane, br, h0, b);
}

#define SLP 2
constexpr int OBUF = 38912, OWO = 18432, LDS2 = 4 * OBUF + 1024;
struct P2 {
  const char* ofr; const char* wo[2]; char* slab; unsigned* cnt; char* out;
};
template <int NQ, int QT0, int NN>
__device__ __forceinline__ void k2_body(const P2& p, int w, int lane, int n0, int br, int b, int nt, int ksp) {
  const int g = lane >> 4, c = lane & 15;
  unsigned offW[3];
  sfor<0, 3>([&](auto I) {
    int o = 1024 * I.v + 16 * lane, rr = o / 320, ch = (o % 320) >> 4;
    offW[I.v] = (8 * w + rr) * 1280 + (((((ch >> 1) ^ ((rr >> 2) & 1)) << 1) | (ch & 1)) << 4);
  });
  const unsigned offO = 2304 * w + 16 * lane;
  const char* ob = p.ofr + (size_t)((br * 8 + b) * 192 + ksp * 48) * 9216;
  const char* wb = (br ? p.wo[1] : p.wo[0]) + (size_t)ksp * 1536 * 1280 + nt * 320;
  auto vm = [&](int i, auto BUF, auto N) {
    constexpr int n = N.v;
    if constexpr (n < 3) {
      unsigned l = BUF.v * OBUF + 2304 * w + n * 1024;
      if constexpr (n < 2) dma16(offO, ob + i * OWO + n * 1024, l);
      else if (lane < 16) dma16(offO, ob + i * OWO + n * 1024, l);
    } else {
      unsigned l = BUF.v * OBUF + OWO + 2560 * w + (n - 3) * 1024;
      if constexpr (n < 5) dma16(offW[n - 3], wb + i * 64 * 1280, l);
      else if (lane < 32) dma16(offW[2], wb + i * 64 * 1280, l);
    }
  };
  const unsigned rowp = OWO + (4 * g + (c >> 2)) * 320 + (c & 3) * 8 + n0 * 32;
  const unsigned aT0 = rowp + ((n0 & 1) ? -(g & 1) * 32 : (g & 1) * 32), aT1 = rowp + ((n0 & 1) ? (g & 1) * 32 : -(g & 1) * 32);
  const unsigned aO = QT0 * 1024 + lane * 16;
  constexpr int NM = NN * NQ, NR = NN + NQ;
  bf16x8 fa[2][NN], fb[2][NQ];
  f32x4 acc[NM];
  sfor<0, NM>([&](auto M) { acc[M.v] = f32x4{0, 0, 0, 0}; });
  lp pO, pT0, pT1;
  auto setp = [&](int bufi) { pO = mkp(aO + bufi * OBUF); pT0 = mkp(aT0 + bufi * OBUF); pT1 = mkp(aT1 + bufi * OBUF); };
  auto rd1 = [&](auto KS, auto R) {
    constexpr int ks = KS.v, r = R.v;
    if constexpr (r == 0) fb[ks][0] = lds128(pO + ks * 9216);
    else if constexpr (r <= NN) fa[ks][r - 1] = ldstr((((r - 1) & 1) ? pT1 : pT0) + (r - 1) * 32 + ks * 10240, 5120);
    else fb[ks][r - NN] = lds128(pO + ks * 9216 + (r - NN) * 1024);
  };
  auto seg = [&](auto KS, auto NI, auto&& item) {
    sfor<0, NM>([&](auto M) {
      acc[M.v] = mma(fa[KS.v][M.v % NN], fb[KS.v][M.v / NN], acc[M.v]);
      __builtin_amdgcn_sched_barrier(0);
      constexpr int per = (NI.v + NM - 1) / NM, lo = M.v * per < NI.v ? M.v * per : NI.v, hi = lo + per < NI.v ? lo + per : NI.v;
      sfor<lo, hi>([&](auto K) { item(K); __builtin_amdgcn_sched_barrier(0); });
    });
  };
  unsigned stv = 0;
  STAMP(0);
  const int grp = (br * 8 + b) * 4 + nt;
  sfor<0, 4>([&](auto S) { sfor<0, 6>([&](auto N) { vm(S.v, S, N); }); });
  if (w == 0) dma4((lane & 31) * 8192 + ((lane >> 3) & 3) * 2048, p.slab + (size_t)grp * 262144, 4 * OBUF);
  else if (w == 1) dma4(((lane * 9) >> 2) * 2560, p.out + (size_t)b * 144 * 2560, 4 * OBUF + 256);
  else if (w == 2) dma4(64, p.cnt + grp * 256, 4 * OBUF + 512);
  wait_vm<18>(); bar();
  setp(0);
  sfor<0, NR>([&](auto R) { rd1(IC<0>{}, R); });
  __builtin_amdgcn_sched_barrier(0);
  auto stage = [&](int i, auto J, auto LAST) {
    constexpr int j = J.v;
    constexpr int pw = (LAST.v && j > 0) ? 0 : 3;
    seg(IC<0>{}, IC<NR + pw>{}, [&](auto K) {
      if constexpr (K.v < NR) rd1(IC<1>{}, K);
      else if (i > 0) vm(i + 3, IC<(j + 3) & 3>{}, IC<3 + K.v - NR>{});
    });
    if constexpr (!LAST.v || j == 0) wait_vm<12>(); else if constexpr (j == 1) wait_vm<6>(); else if constexpr (j == 2) wait_vm<0>();
    bar();
    STAMP(1 + i);
    constexpr bool fin = LAST.v && j == 3;
    if constexpr (!fin) setp((j + 1) & 3);
    seg(IC<1>{}, IC<(fin ? 0 : NR) + (LAST.v ? 0 : 3)>{}, [&](auto K) {
      if constexpr (K.v < NR) rd1(IC<0>{}, K); else vm(i + 4, J, IC<K.v - NR>{});
    });
  };
#pragma unroll 1
  for (int it = 0; it < 5; ++it) sfor<0, 4>([&](auto J) { stage(4 * it + J.v, J, IC<0>{}); });
  sfor<0, 4>([&](auto J) { stage(20 + J.v, J, IC<1>{}); });
  STAMP(25);
  char* sl = p.slab + (size_t)(grp * 32 + w) * 8192 + lane * 16;
  unsigned* fl = p.cnt + (grp * 8 + w) * 32;
  auto tail = [&](auto KSP) {
    constexpr int ks = KSP.v;
    float mf = 0.f;
    sfor<0, NM>([&](auto M) { if constexpr ((M.v & 3) != ks) {
      mf = fmaxf(mf, fmaxf(fmaxf(fabsf(acc[M.v][0]), fabsf(acc[M.v][1])), fmaxf(fabsf(acc[M.v][2]), fabsf(acc[M.v][3])))); } });
    unsigned e = (wave_umax(__float_as_uint(mf)) >> 23) + 1;
    e = e > 253u ? 253u : e;
    const float inv = __uint_as_float((254u - e) << 23);
    sfor<0, 8>([&](auto I) {
      constexpr int d = I.v >> 1, m0 = d + 8 * (I.v & 1), m1 = m0 + 4;
      if constexpr (d != ks && m0 < NM) {
        f32x4 x = acc[m0] * inv;
        char* a = sl + (ks * 32 + d) * 2048 + (I.v & 1) * 1024;
        if constexpr (m1 < NM) { f32x4 y = acc[m1] * inv; st_sc1(a, u32x4{pkn(x[0], x[1]), pkn(x[2], x[3]), pkn(y[0], y[1]), pkn(y[2], y[3])}); }
        else st2_sc1(a, u32x2{pkn(x[0], x[1]), pkn(x[2], x[3])});
      }
    });
    STAMP(26);
    wait_vm<0>();
    STAMP(27);
    if (lane == 0) __hip_atomic_store(fl + ks, e, __ATOMIC_RELAXED, __HIP_MEMORY_SCOPE_AGENT);
    unsigned f;
    do { __builtin_amdgcn_s_sleep(SLP); f = __hip_atomic_load(fl + (lane & 3), __ATOMIC_RELAXED, __HIP_MEMORY_SCOPE_AGENT); } while (__builtin_amdgcn_ballot_w64(f == 0u) != 0ull);
    STAMP(28);
    u32x4 r4[2][3]; u32x2 r2[3];
    sfor<0, 6>([&](auto I) {
      constexpr int o = I.v >> 1, h = I.v & 1, src = o < ks ? o : o + 1, m0 = ks + 8 * h, m1 = m0 + 4;
      const char* a = sl + (src * 32 + ks) * 2048 + h * 1024;
      if constexpr (m1 < NM) ld_sc1(r4[h][o], a); else if constexpr (m0 < NM) ld2_sc1(r2[o], a);
    });
    float sc[3];
    sfor<0, 3>([&](auto O) { constexpr int src = O.v < ks ? O.v : O.v + 1;
      sc[O.v] = __uint_as_float(__builtin_amdgcn_readlane(f, src) << 23) * (1.f / 32767.f); });
    __builtin_amdgcn_sched_barrier(0);
    wait_vm<0>();
    sfor<0, 6>([&](auto I) {
      constexpr int o = I.v >> 1, h = I.v & 1, m0 = ks + 8 * h, m1 = m0 + 4;
      if constexpr (m1 < NM) pinu4(r4[h][o]); else if constexpr (m0 < NM) pinu2(r2[o]);
    });
    sfor<0, 4>([&](auto K) {
      constexpr int m = ks + 4 * K.v, h = K.v >> 1;
      if constexpr (m < NM) {
        f32x4 v[4];
        sfor<0, 4>([&](auto S) {
          if constexpr (S.v == ks) v[S.v] = acc[m];
          else {
            constexpr int o = S.v < ks ? S.v : S.v - 1;
            int a0, a1;
            if constexpr (ks + 8 * h + 4 < NM) { a0 = r4[h][o][2 * (K.v & 1)]; a1 = r4[h][o][2 * (K.v & 1) + 1]; } else { a0 = r2[o][0]; a1 = r2[o][1]; }
            v[S.v] = f32x4{(float)(short)a0, (float)(a0 >> 16), (float)(short)a1, (float)(a1 >> 16)} * sc[o];
          }
        });
        f32x4 o = (v[0] + v[1]) + (v[2] + v[3]);
        *(u32x2*)(p.out + ((size_t)(b * 144 + 16 * (QT0 + m / NN) + c) * 1280 + br * 640 + nt * 160 + 16 * (n0 + m % NN) + 4 * g) * 2) =
            u32x2{pk2(o[0], o[1]), pk2(o[2], o[3])};
      }
    });
  };
  if (ksp == 0) tail(IC<0>{}); else if (ksp == 1) tail(IC<1>{}); else if (ksp == 2) tail(IC<2>{}); else tail(IC<3>{});
  STAMP(29);
}
__global__ __launch_bounds__(512, 2) void k2_oproj(P2 p) {
  extern __shared__ __attribute__((aligned(16))) char smem[];
  const int w = __builtin_amdgcn_readfirstlane(threadIdx.x >> 6), lane = threadIdx.x & 63;
  const int xcd = blockIdx.x & 7, idx = blockIdx.x >> 3;
  const int br = xcd >> 2, b = idx >> 2, nt = idx & 3, ksp = xcd & 3;
  if (w < 2) k2_body<5, 0, 3>(p, w, lane, 3 * w, br, b, nt, ksp);
  else if (w < 4) k2_body<5, 0, 2>(p, w, lane, 2 * w + 2, br, b, nt, ksp);
  else if (w < 6) k2_body<4, 5, 2>(p, w, lane, 2 * w - 8, br, b, nt, ksp);
  else k2_body<4, 5, 3>(p, w, lane, 3 * w - 14, br, b, nt, ksp);
}

extern "C" void kernel_launch(void* const* d_in, const int* in_sizes, int n_in, void* d_out, int out_size, void* d_ws,
                              size_t ws_size, hipStream_t stream) {
  P1 p;
  p.x[0] = (const char*)d_in[0]; p.k[0] = (const char*)d_in[1]; p.v[0] = (const char*)d_in[2]; p.wq[0] = (const char*)d_in[3];
  p.x[1] = (const char*)d_in[5]; p.k[1] = (const char*)d_in[6]; p.v[1] = (const char*)d_in[7]; p.wq[1] = (const char*)d_in[8];
  p.wo[0] = (const char*)d_in[4]; p.wo[1] = (const char*)d_in[9];
  p.ofr = (char*)d_ws;
  p.cnt = (unsigned*)((char*)d_ws + (30u << 20));
  P2 q;
  q.ofr = (const char*)d_ws; q.wo[0] = (const char*)d_in[4]; q.wo[1] = (const char*)d_in[9];
  q.slab = (char*)d_ws + (32u << 20); q.cnt = p.cnt; q.out = (char*)d_out;
  (void)hipFuncSetAttribute((const void*)k1_attn, hipFuncAttributeMaxDynamicSharedMemorySize, LDS1);
  (void)hipFuncSetAttribute((const void*)k2_oproj, hipFuncAttributeMaxDynamicSharedMemorySize, LDS2);
  k1_attn<<<256, 512, LDS1, stream>>>(p);
  k2_oproj<<<256, 512, LDS2, stream>>>(q);
}
```

```cpp
#pragma clang diagnostic ignored "-Wint-to-pointer-cast"
#include <hip/hip_runtime.h>
#include <hip/hip_bf16.h>
#include <stdint.h>

typedef __attribute__((ext_vector_type(8))) short bf16x8;
typedef __attribute__((ext_vector_type(4))) short bf16x4;
typedef __attribute__((ext_vector_type(4))) float f32x4;
typedef __attribute__((ext_vector_type(2))) float f32x2;
typedef __attribute__((ext_vector_type(2))) __bf16 bf2;
typedef __attribute__((ext_vector_type(2))) unsigned u32x2;
typedef __attribute__((ext_vector_type(4))) unsigned u32x4;
#define LDSP(T) __attribute__((address_space(3))) T*

constexpr int SQ = 144, DM = 640, NH = 32, SK = 512, DH = 192, HD = 6144;
constexpr int XBUF = 18432, SLOT = 24576, RING = 55296, STATM = 153600, STATS = 158208, LDS1 = 162816;

template <int I> struct IC { static constexpr int v = I; };
template <int B, int E, class F> __device__ __forceinline__ void sfor(F&& f) {
  if constexpr (B < E) { f(IC<B>{}); sfor<B + 1, E>(f); }
}
template <int N> __device__ __forceinline__ void wait_vm() { asm volatile("s_waitcnt vmcnt(%0)" ::"i"(N) : "memory"); }
__device__ __forceinline__ void bar() {
  __builtin_amdgcn_sched_barrier(0);
  asm volatile("s_waitcnt lgkmcnt(0)\n\ts_barrier" ::: "memory");
  __builtin_amdgcn_sched_barrier(0);
}
__device__ __forceinline__ void dma16(unsigned voff, const void* base, unsigned lds) {
  asm volatile("s_mov_b32 m0, %1\n\ts_nop 0\n\tglobal_load_lds_dwordx4 %0, %2" ::"v"(voff), "s"(lds), "s"(base) : "memory");
}
__device__ __forceinline__ void dma16nt(unsigned voff, const void* base, unsigned lds) {
  asm volatile("s_mov_b32 m0, %1\n\ts_nop 0\n\tglobal_load_lds_dwordx4 %0, %2 nt" ::"v"(voff), "s"(lds), "s"(base) : "memory");
}
template <int OFF> __device__ __forceinline__ void gload_nt(bf16x8& d, unsigned voff, const void* base) {
  asm volatile("global_load_dwordx4 %0, %1, %2 offset:%3 nt" : "=v"(d) : "v"(voff), "s"(base), "i"(OFF) : "memory");
}
#define LD3 "s_cmp_lg_u32 %8, 0\n\ts_cbranch_scc1 .Lk%=\n\tglobal_load_dwordx4 %0, %3, %4 offset:%5 nt\n\t" \
            "global_load_dwordx4 %1, %3, %4 offset:%6 nt\n\tglobal_load_dwordx4 %2, %3, %4 offset:%7 nt\n.Lk%=:"
template <int OFF> __device__ __forceinline__ void gload3_if(bf16x8& d0, bf16x8& d1, bf16x8& d2, unsigned voff, const void* base, int sk) {
  asm volatile(LD3 : "=&v"(d0), "=&v"(d1), "=&v"(d2) : "v"(voff), "s"(base), "i"(OFF), "i"(OFF + 128), "i"(OFF + 256), "s"(sk) : "memory", "scc");
}
template <int OFF> __device__ __forceinline__ void gload3_keep(bf16x8& d0, bf16x8& d1, bf16x8& d2, unsigned voff, const void* base, int sk) {
  asm volatile(LD3 : "+v"(d0), "+v"(d1), "+v"(d2) : "v"(voff), "s"(base), "i"(OFF), "i"(OFF + 128), "i"(OFF + 256), "s"(sk) : "memory", "scc");
}
__device__ __forceinline__ void undef8(bf16x8& d) { asm volatile("" : "=v"(d)); }
__device__ __forceinline__ void dma4(unsigned voff, const void* base, unsigned lds) {
  asm volatile("s_mov_b32 m0, %1\n\ts_nop 0\n\tglobal_load_lds_dword %0, %2" ::"v"(voff), "s"(lds), "s"(base) : "memory");
}
template <class T> __device__ __forceinline__ void ost(char* a, T v) { *(T*)a = v; }
__device__ __forceinline__ void st2_sc1(char* a, u32x2 v) { asm volatile("global_store_dwordx2 %0, %1, off sc1\n\ts_nop 1" ::"v"(a), "v"(v) : "memory"); }
__device__ __forceinline__ void ld2_sc1(u32x2& d, const char* a) { asm volatile("global_load_dwordx2 %0, %1, off sc1" : "=v"(d) : "v"(a) : "memory"); }
__device__ __forceinline__ void pinu4(u32x4& d) { asm volatile("" : "+v"(d)); }
__device__ __forceinline__ void pinu2(u32x2& d) { asm volatile("" : "+v"(d)); }
__device__ __forceinline__ unsigned pkn(float a, float b) { return __builtin_bit_cast(unsigned, __builtin_amdgcn_cvt_pknorm_i16(a, b)); }
__device__ __forceinline__ unsigned umax(unsigned a, unsigned b) { return a > b ? a : b; }
__device__ __forceinline__ unsigned wave_umax(unsigned v) {
  v = umax(v, __builtin_amdgcn_update_dpp(v, v, 0x121, 0xF, 0xF, false));
  v = umax(v, __builtin_amdgcn_update_dpp(v, v, 0x122, 0xF, 0xF, false));
  v = umax(v, __builtin_amdgcn_update_dpp(v, v, 0x124, 0xF, 0xF, false));
  v = umax(v, __builtin_amdgcn_update_dpp(v, v, 0x128, 0xF, 0xF, false));
  auto r = __builtin_amdgcn_permlane16_swap(v, v, false, false);
  v = umax(r[0], r[1]);
  auto t = __builtin_amdgcn_permlane32_swap(v, v, false, false);
  return __builtin_amdgcn_readfirstlane(umax(t[0], t[1]));
}
__device__ __forceinline__ void st_sc1(char* a, u32x4 v) { asm volatile("global_store_dwordx4 %0, %1, off sc1\n\ts_nop 1" ::"v"(a), "v"(v) : "memory"); }
__device__ __forceinline__ void ld_sc1(u32x4& d, const char* a) { asm volatile("global_load_dwordx4 %0, %1, off sc1" : "=v"(d) : "v"(a) : "memory"); }
__device__ __forceinline__ void pin(bf16x8& d) { asm volatile("" : "+v"(d)); }
__device__ __forceinline__ void pinf(float& d) { asm volatile("" : "+v"(d)); }
__device__ __forceinline__ unsigned opq(unsigned d) { asm volatile("" : "+v"(d)); return d; }
__device__ __forceinline__ unsigned lid() {
  unsigned d; asm volatile("v_mbcnt_lo_u32_b32 %0, -1, 0\n\tv_mbcnt_hi_u32_b32 %0, -1, %0" : "=v"(d)); return d;
}
typedef LDSP(char) lp;
__device__ __forceinline__ lp mkp(unsigned a) { return (lp)opq(a); }
__device__ __forceinline__ bf16x8 lds128(lp a) { return *(LDSP(bf16x8))a; }
__device__ __forceinline__ bf16x8 ldstr(lp a, int d) {
  bf16x4 lo = __builtin_amdgcn_ds_read_tr16_b64_v4i16((LDSP(bf16x4))a);
  bf16x4 hi = __builtin_amdgcn_ds_read_tr16_b64_v4i16((LDSP(bf16x4))(a + d));
  return __builtin_shufflevector(lo, hi, 0, 1, 2, 3, 4, 5, 6, 7);
}
__device__ __forceinline__ unsigned pk2(float a, float b) {
  f32x2 v = {a, b};
  return __builtin_bit_cast(unsigned, __builtin_convertvector(v, bf2));
}
__device__ __forceinline__ f32x4 mma(bf16x8 a, bf16x8 b, f32x4 c) {
  typedef __attribute__((ext_vector_type(8))) __bf16 b8;
  return __builtin_amdgcn_mfma_f32_16x16x32_bf16(__builtin_bit_cast(b8, a), __builtin_bit_cast(b8, b), c, 0, 0, 0);
}
__device__ __forceinline__ void kfix(bf16x8& a, bf16x8& b) {
  u32x4 x = __builtin_bit_cast(u32x4, a), y = __builtin_bit_cast(u32x4, b), p, q;
  for (int i = 0; i < 4; ++i) {
    p[i] = __builtin_amdgcn_update_dpp(x[i], y[i], 0x128, 0xF, 0xC, false);
    q[i] = __builtin_amdgcn_update_dpp(y[i], x[i], 0x128, 0xF, 0x3, false);
  }
  a = __builtin_bit_cast(bf16x8, p); b = __builtin_bit_cast(bf16x8, q);
}
__device__ __forceinline__ float max3(float a, float b, float c) { float d; asm("v_max3_f32 %0, %1, %2, %3" : "=v"(d) : "v"(a), "v"(b), "v"(c)); return d; }
template <int OFF> __device__ __forceinline__ void lds_max(lp a, float v) { asm volatile("ds_max_f32 %0, %1 offset:%2" ::"v"(a), "v"(v), "i"(OFF) : "memory"); }
__device__ __forceinline__ float bfly_sum(float a, float b, float c, float d) {
  auto r = __builtin_amdgcn_permlane16_swap(__float_as_uint(a), __float_as_uint(b), false, false);
  auto s = __builtin_amdgcn_permlane16_swap(__float_as_uint(c), __float_as_uint(d), false, false);
  float ab = __uint_as_float(r[0]) + __uint_as_float(r[1]), cd = __uint_as_float(s[0]) + __uint_as_float(s[1]);
  auto t = __builtin_amdgcn_permlane32_swap(__float_as_uint(ab), __float_as_uint(cd), false, false);
  return __uint_as_float(t[0]) + __uint_as_float(t[1]);
}
struct P1 {
  const char* x[2]; const char* k[2]; const char* v[2]; const char* wq[2];
  const char* wo[2];
  unsigned* cnt;
  char* ofr;
};

template <int NQ, int QT0, int HALF>
__device__ __forceinline__ void k1_body(const P1& p, int w, int lane, int br, int h0, int b) {
  const int dq = w & 3;
  unsigned offW[3], offV[3], offX[3], offK;
  auto mkWV = [&](unsigned* off, int rs) {
    const unsigned l = lid();
    sfor<0, 3>([&](auto I) {
      unsigned t = 64 * I.v + l, rr = (t * 2731u) >> 16, ch = t - 24 * rr;
      unsigned hh = ((rr >> 1) & 1) | ((w & 1) << 1);
      off[I.v] = (8 * w + rr) * rs + ((ch ^ (hh << 1)) << 4);
    });
  };
  auto mkX = [&]() {
    const unsigned l = lid();
    sfor<0, 3>([&](auto I) {
      unsigned row = 18 * w + 8 * I.v + (l >> 3);
      offX[I.v] = row * (DM * 2) + (((l & 7) ^ ((row >> 1) & 7)) << 4);
    });
  };
  auto mkK = [&]() { const unsigned l = lid(); offK = (16 * w + (l & 7)) * 384 + (l & 8) * 8 + (l & 48); };
  const unsigned ldsW = RING + w * 3072, ldsX = w * 2304;
  lp adT[3];
  int ut[3];
  sfor<0, 3>([&](auto I) { ut[I.v] = 3 * dq + ((dq & 1) ? (I.v + 1) % 3 : I.v); });
  auto mkT = [&]() {
    const unsigned l = lid(), q4 = (l >> 2) & 3, hh = (q4 >> 1) | ((l >> 3) & 2);
    sfor<0, 3>([&](auto I) { adT[I.v] = mkp(RING + (8 * (l >> 4) + q4) * 384 + ((ut[I.v] ^ hh) << 5) + ((l & 3) << 3)); });
  };
  lp adX[2];
  auto mkXr = [&]() {
    const unsigned l = lid(), cc = l & 15, gg = l >> 4;
    adX[0] = mkp((QT0 * 16 + cc) * 128 + ((gg ^ (cc >> 1)) << 4));
    adX[1] = mkp((QT0 * 16 + cc) * 128 + (((4 + gg) ^ (cc >> 1)) << 4));
  };
  auto mkH = [&]() { const unsigned l = lid(); return ((l >> 5) * 16 + (l & 15)) * 16 + ((l >> 1) & 8); };
  mkWV(offW, HD * 2); mkX(); mkK(); mkT();
  const lp adL = mkp(lane * 16);
  const float CS = 0.07216878364870322f * 1.4426950408889634f;

  auto dW = [&](const char* wb, int i, auto I) {
    dma16(offW[I.v], wb + (size_t)i * 64 * HD * 2, ldsW + (i & 3) * SLOT + I.v * 1024); };
  auto dX = [&](const char* xb, int i, auto I) {
    unsigned l = ldsX + ((i + 2) % 3) * XBUF + I.v * 1024;
    if constexpr (I.v < 2) dma16(offX[I.v], xb + i * 128, l);
    else if (lid() < 16) dma16(offX[2], xb + i * 128, l);
  };
  auto dV = [&](const char* vb, int i, auto I) {
    dma16nt(offV[I.v], vb + i * SLOT, ldsW + (i & 3) * SLOT + I.v * 1024); };
  auto issueW = [&](const char* wb, int i) { sfor<0, 3>([&](auto I) { dW(wb, i, I); }); };
  auto issueX = [&](const char* xb, int i) { sfor<0, 3>([&](auto I) { dX(xb, i, I); }); };
  constexpr int NST = 2 * NQ, NM = 3 * NQ, NR = 3 + NQ;

  const char* xb = (br ? p.x[1] : p.x[0]) + (size_t)b * SQ * DM * 2;
  const char* wb0 = (br ? p.wq[1] : p.wq[0]) + (size_t)h0 * DH * 2;
  const size_t kvo = (size_t)(b * NH + h0) * SK * DH * 2;
  const char* kb0 = (br ? p.k[1] : p.k[0]) + kvo;
  const char* vb0 = (br ? p.v[1] : p.v[0]) + kvo;
  bf16x8 kf[4][6];
  auto dKn = [&](const char* kb, int sk, auto G) {
    constexpr int jt = G.v >> 1, hf = G.v & 1;
    gload3_if<hf * 3072>(kf[jt][hf], kf[jt][2 + hf], kf[jt][4 + hf], offK, kb + jt * 49152, sk);
  };
  auto dK0 = [&](const char* kb, int sk, auto G) {
    constexpr int jt = G.v >> 1, hf = G.v & 1;
    gload3_keep<hf * 3072>(kf[jt][hf], kf[jt][2 + hf], kf[jt][4 + hf], offK, kb + jt * 49152, sk);
  };
  auto dK3 = [&](const char* kb, auto M) { gload_nt<(M.v % 3) * 128 + (M.v / 3) * 3072>(kf[3][2 * (M.v % 3) + M.v / 3], offK, kb + 3 * 49152); };
  const unsigned ldsT = STATM + 1024 + w * 256;
  auto touchW = [&](const char* wb) {
    unsigned l = lid(); l = l < 60 ? l : 59;
    unsigned r = (l * 43u) >> 8;
    dma4((80 * b + 10 * w + r) * (HD * 2) + (l - 6 * r) * 64, wb, ldsT);
  };
  const int late = b & 1;
  touchW(wb0);
  { unsigned l = lid(); l = l < 45 ? l : 44;
    dma4((h0 & 3) * 46080 + (90 * w + l) * 64, xb, ldsT); dma4((h0 & 3) * 46080 + (90 * w + 45 + l) * 64, xb, ldsT); }
  sfor<0, 6>([&](auto S) { undef8(kf[0][S.v]); undef8(kf[1][S.v]); undef8(kf[2][S.v]); });
  sfor<0, 6>([&](auto M) { dK0(kb0, late, M); });
  issueW(wb0, 0); issueX(xb, 0); issueW(wb0, 1); issueX(xb, 1);
  wait_vm<6>(); bar();
#pragma unroll 1
  for (int u = 0; u < 2; ++u) {
    const char* wb = wb0 + u * (4 * DH * 2);
    const char* kb = kb0 + u * (4 * SK * DH * 2);
    const char* vb = vb0 + u * (4 * SK * DH * 2);
    const int nk0 = u | (late ^ 1), nk1 = (u & late) ^ 1, nk2 = u ^ late ^ 1;
    bf16x8 fa[2][3], fb[2][NQ];
    f32x4 acc[3][NQ];
    auto rdT = [&](auto S, auto KS, auto T) {
      constexpr int i = S.v, ks = KS.v;
      if constexpr ((i & 3) == 3) fa[ks][T.v] = ldstr(mkp((unsigned)(size_t)adT[T.v] + 3 * SLOT) + ks * 12288, 1536);
      else fa[ks][T.v] = ldstr(adT[T.v] + (i & 3) * SLOT + ks * 12288, 1536);
    };
    auto seg = [&](auto KS, auto NA, auto&& ia, auto NB, auto&& ib, auto NC, auto&& ic) {
      sfor<0, NM>([&](auto M) {
        constexpr int m = M.v, q = m / 3, t = m % 3;
        acc[t][q] = mma(fa[KS.v][t], fb[KS.v][q], acc[t][q]);
        __builtin_amdgcn_sched_barrier(0);
        if constexpr (m < NA.v) { ia(M); __builtin_amdgcn_sched_barrier(0); }
        sfor<(m * NB.v + NM - 1) / NM, ((m + 1) * NB.v + NM - 1) / NM>([&](auto K) { ib(K); __builtin_amdgcn_sched_barrier(0); });
        sfor<(m * NC.v + NM / 2) / NM, ((m + 1) * NC.v + NM / 2) / NM>([&](auto K) { ic(K); __builtin_amdgcn_sched_barrier(0); });
      });
    };
    auto nop = [&](auto) {};
    {
      sfor<0, 3>([&](auto T) { sfor<0, NQ>([&](auto Q) { acc[T.v][Q.v] = f32x4{0, 0, 0, 0}; }); });
      auto rd1 = [&](auto S, auto KS, auto R) {
        if constexpr (R.v == 0) fb[KS.v][0] = lds128(adX[KS.v] + ((S.v + 2) % 3) * XBUF);
        else if constexpr (R.v <= 3) rdT(S, KS, IC<R.v - 1>{});
        else fb[KS.v][R.v - 3] = lds128(adX[KS.v] + ((S.v + 2) % 3) * XBUF + (R.v - 3) * 2048);
      };
      auto vm = [&](auto J, auto N) {
        constexpr int j = J.v, n = N.v;
        if constexpr (j < 0) { if (u == 0) { if constexpr (n == 1) issueX(xb, 2); else issueW(wb, 2 + n / 2); } }
        else if constexpr (j <= 5) { if constexpr (n < 3) dX(xb, j + 3, N); else if constexpr (n < 6) dW(wb, j + 4, IC<n - 3>{}); else dK0(kb, n < 10 ? nk0 : nk1, IC<n - 6>{}); }
        else if constexpr (j == 6) { if constexpr (n < 3) dX(xb, 9, N); else dK0(kb, nk2, IC<4>{}); }
        else if constexpr (j == 7) dK0(kb, nk2, IC<5>{});
        else dK3(kb, N);
      };
      mkXr();
      sfor<0, NR>([&](auto R) { rd1(IC<0>{}, IC<0>{}, R); });
      __builtin_amdgcn_sched_barrier(0);
      sfor<0, 10>([&](auto S) {
        constexpr int i = S.v;
        constexpr int pv = i == 0 ? 3 : i == 4 ? 12 : i <= 6 ? 6 : i == 7 ? 4 : i == 8 ? 1 : 3, p1 = i == 0 ? 0 : (pv + 1) / 2;
        seg(IC<0>{}, IC<NR>{}, [&](auto R) { rd1(S, IC<1>{}, R); }, IC<pv - p1>{}, [&](auto K) { vm(IC<i - 1>{}, IC<p1 + K.v>{}); },
            IC<(i == 9 ? 3 : 0)>{}, [&](auto L) { kfix(kf[1][2 * L.v], kf[1][2 * L.v + 1]); pin(kf[1][2 * L.v]); pin(kf[1][2 * L.v + 1]); });
        if constexpr (i == 0) { if (u == 0) wait_vm<9>(); else wait_vm<9 + NST>(); }
        else if constexpr (i == 1) { if (u == 0) wait_vm<9>(); else wait_vm<6 + NST>(); }
        else if constexpr (i == 4 || i == 5) { if (nk0 == 0) wait_vm<21>(); else if (nk1 == 0) wait_vm<15>(); else wait_vm<9>(); }
        else if constexpr (i <= 6) wait_vm<9>();
        else if constexpr (i == 7) { if (nk2 == 0) wait_vm<9>(); else wait_vm<6>(); }
        else if constexpr (i == 8) { if (nk2 == 0) wait_vm<6>(); else wait_vm<0>(); }
        bar();
        if constexpr (i == 8) { sfor<0, 6>([&](auto S2) { pin(kf[0][S2.v]); pin(kf[1][S2.v]); }); __builtin_amdgcn_sched_barrier(0); }
        constexpr int cv = i == 3 ? 12 : i <= 5 ? 6 : i == 6 ? 4 : i == 7 ? 1 : i == 8 ? 3 : 0;
        seg(IC<1>{}, IC<(i < 9 ? NR : 0)>{}, [&](auto R) { rd1(IC<i + 1>{}, IC<0>{}, R); }, IC<(cv + 1) / 2>{}, [&](auto K) { vm(S, K); },
            IC<(i == 8 ? 3 : 0)>{}, [&](auto L) { kfix(kf[0][2 * L.v], kf[0][2 * L.v + 1]); pin(kf[0][2 * L.v]); pin(kf[0][2 * L.v + 1]); });
      });
      mkWV(offV, 384);
      if (w < 3) *(LDSP(float))(mkp(STATM + w * 256 + (lid() << 2))) = -__builtin_inff();
      const unsigned adH = mkH();
      sfor<0, 3>([&](auto T) {
        lp a = mkp(adH + (ut[T.v] >> 1) * 1024 + (ut[T.v] & 1) * 512) + QT0 * 6144;
        sfor<0, NQ>([&](auto Q) {
          f32x4 v = acc[T.v][Q.v];
          *(LDSP(u32x2))(a + Q.v * 6144) = u32x2{pk2(v[0], v[1]), pk2(v[2], v[3])};
        });
      });
      bar();
    }
    f32x4 sa[4][9];
    float mx[9];
    const lp adM = mkp((lid() & 15) * 48 + STATM);
    {
      constexpr int PF = 4;
      bf16x8 qf[PF + 1];
      auto rowmax = [&](auto PR, auto Q) {
        f32x4 a = sa[2 * PR.v][Q.v], bb = sa[2 * PR.v + 1][Q.v];
        float m = max3(max3(a[0], a[1], a[2]), max3(a[3], bb[0], bb[1]), bb[2]);
        if constexpr (PR.v == 0) mx[Q.v] = max3(m, bb[3], bb[3]);
        else lds_max<Q.v * 4>(adM, max3(m, bb[3], mx[Q.v]));
      };
      sfor<0, PF>([&](auto N) { qf[N.v] = lds128(adL + N.v * 1024); });
      __builtin_amdgcn_sched_barrier(0);
      sfor<0, 108>([&](auto N) {
        constexpr int n = N.v, pr = n / 54, q = (n % 54) / 6, s = n % 6;
        if constexpr (n == 54) {
          wait_vm<5>();
          sfor<0, 6>([&](auto S) { pin(kf[2][S.v]); pin(kf[3][S.v]); });
          __builtin_amdgcn_sched_barrier(0);
        }
        if constexpr (n == 54 || n == 56 || n == 58) {
          kfix(kf[2][s], kf[2][s + 1]); kfix(kf[3][s], kf[3][s + 1]);
          __builtin_amdgcn_sched_barrier(0);
        }
        if constexpr (s == 0) { sa[2 * pr][q] = f32x4{0, 0, 0, 0}; sa[2 * pr + 1][q] = f32x4{0, 0, 0, 0}; }
        sa[2 * pr][q] = mma(kf[2 * pr][s], qf[n % (PF + 1)], sa[2 * pr][q]);
        __builtin_amdgcn_sched_barrier(0);
        if constexpr (n + PF < 108) qf[(n + PF) % (PF + 1)] = lds128(adL + ((n + PF) % 54) * 1024);
        __builtin_amdgcn_sched_barrier(0);
        sa[2 * pr + 1][q] = mma(kf[2 * pr + 1][s], qf[n % (PF + 1)], sa[2 * pr + 1][q]);
        __builtin_amdgcn_sched_barrier(0);
        if constexpr (n < 20 && n % 6 == 2) { dK3(kb, IC<3 + n / 6>{}); __builtin_amdgcn_sched_barrier(0); }
        if constexpr (n >= 20 && n < 104 && (n - 20) % 7 == 0) { dV(vb, (n - 20) / 21, IC<((n - 20) / 7) % 3>{}); __builtin_amdgcn_sched_barrier(0); }
        if constexpr (n == 103) { if (u == 0) touchW(wb0 + 4 * DH * 2); __builtin_amdgcn_sched_barrier(0); }
        if constexpr (s == 2 && n >= 6) { constexpr int pn = n - 6; rowmax(IC<pn / 54>{}, IC<(pn % 54) / 6>{}); __builtin_amdgcn_sched_barrier(0); }
      });
      asm volatile("s_nop 7\n\ts_nop 7\n\ts_nop 7");
      __builtin_amdgcn_sched_barrier(0);
      rowmax(IC<1>{}, IC<8>{});
    }
    bar();
    float sm[9];
    {
      f32x4 m0 = *(LDSP(f32x4))(adM), m1 = *(LDSP(f32x4))(adM + 16);
      float m2 = *(LDSP(float))(adM + 32);
      sfor<0, 4>([&](auto Q) { mx[Q.v] = -CS * m0[Q.v]; mx[4 + Q.v] = -CS * m1[Q.v]; });
      mx[8] = -CS * m2;
    }
    const lp adP = mkp(mkH() + (((w & 3) >> 1) * 9) * 1024 + (w & 1) * 512);
    auto exph = [&](auto J, auto K) {
      constexpr int j = J.v, pb = ((2 * j + HALF) % 3) * XBUF, q = K.v / 2;
      if constexpr (K.v >= 18) {
        constexpr int k = K.v - 18;
        float t;
        if constexpr (k < 2) t = bfly_sum(sm[4 * k], sm[4 * k + 1], sm[4 * k + 2], sm[4 * k + 3]);
        else t = bfly_sum(sm[8], sm[8], sm[8], sm[8]);
        unsigned l = lid();
        *(LDSP(float))(mkp(STATS + (l & 15) * 32 + (k < 2 ? (l >> 4) * 512 : 0) + w * 4) + k * 2048) = t;
      } else if constexpr ((K.v & 1) == 0) {
        f32x4 v = sa[j][q];
        f32x2 c2 = {CS, CS}, m2 = {mx[q], mx[q]};
        f32x2 lo = __builtin_elementwise_fma(f32x2{v[0], v[1]}, c2, m2), hi = __builtin_elementwise_fma(f32x2{v[2], v[3]}, c2, m2);
        sa[j][q] = f32x4{__builtin_amdgcn_exp2f(lo[0]), __builtin_amdgcn_exp2f(lo[1]), __builtin_amdgcn_exp2f(hi[0]), __builtin_amdgcn_exp2f(hi[1])};
      } else {
        f32x4 e = sa[j][q];
        const unsigned p0 = pk2(e[0], e[1]), p1 = pk2(e[2], e[3]);
        const bf2 one = __builtin_bit_cast(bf2, 0x3f803f80u);
        sm[q] = __builtin_amdgcn_fdot2_f32_bf16(__builtin_bit_cast(bf2, p0), one, j == 0 ? 0.f : sm[q], false);
        sm[q] = __builtin_amdgcn_fdot2_f32_bf16(__builtin_bit_cast(bf2, p1), one, sm[q], false);
        pinf(sm[q]);
        *(LDSP(u32x2))(adP + pb + q * 1024) = u32x2{p0, p1};
      }
    };
    sfor<0, 18>([&](auto K) { exph(IC<0>{}, K); });
    __builtin_amdgcn_sched_barrier(0);
    mkT();
    wait_vm<9>();
    bar();
    {
      const char* wn = wb0 + 4 * DH * 2; const char* xn = xb;
      sfor<0, 3>([&](auto T) { sfor<0, NQ>([&](auto Q) { acc[T.v][Q.v] = f32x4{0, 0, 0, 0}; }); });
      auto rd1 = [&](auto S, auto KS, auto R) {
        if constexpr (R.v == 0) fb[KS.v][0] = lds128(adL + (S.v % 3) * XBUF + (KS.v * 9 + QT0) * 1024);
        else if constexpr (R.v <= 3) rdT(S, KS, IC<R.v - 1>{});
        else fb[KS.v][R.v - 3] = lds128(adL + (S.v % 3) * XBUF + (KS.v * 9 + QT0 + R.v - 3) * 1024);
      };
      const char* kn = kb0 + 4 * SK * DH * 2;
      auto vm = [&](auto J, auto N) {
        constexpr int j = J.v, n = N.v;
        if constexpr (j <= 3) dV(vb, j + 4, N);
        else if constexpr (n == 0) dKn(kn, u, IC<j - 4>{});
        else if (u == 0) { if constexpr (n == 1) issueW(wn, j - 4); else issueX(xn, j - 5); }
        else if constexpr (j == 4) {
          unsigned l = lid(); l = (l < 60 ? l : 59) * 64 + ((blockIdx.x >> 3) * 16 + w * 2) * 3840;
          const char* wo = (br ? p.wo[1] : p.wo[0]) + (size_t)(blockIdx.x & 3) * 1536 * 1280;
          dma4(l, wo, ldsT); dma4(l + 3840, wo, ldsT);
        }
      };
      float rs[NQ];
      f32x4 s0[NQ], s1[NQ];
      auto rsr = [&](auto Q) {
        const lp adS = mkp((lid() & 15) * 32 + STATS + QT0 * 512);
        s0[Q.v] = *(LDSP(f32x4))(adS + Q.v * 512); s1[Q.v] = *(LDSP(f32x4))(adS + Q.v * 512 + 16);
      };
      auto rsc = [&](auto Q) {
        f32x4 t = s0[Q.v] + s1[Q.v];
        rs[Q.v] = __builtin_amdgcn_rcpf((t[0] + t[1]) + (t[2] + t[3]));
        pinf(rs[Q.v]);
      };
      auto pvseg = [&](auto SG, auto NRD, auto&& rd, auto NV, auto&& ex) {
        constexpr int t = SG.v + 5 - 2 * HALF, j = t / 4, pt = t % 4;
        constexpr bool on = t >= 4 && t < 16;
        constexpr int e0 = pt == 0 ? 0 : pt == 1 ? 5 : pt == 2 ? 9 : 14, e1 = pt == 0 ? 5 : pt == 1 ? 9 : pt == 2 ? 14 : (j == 3 ? 21 : 18);
        constexpr int ne = on ? e1 - e0 : SG.v == 15 ? NQ : 0;
        auto eb = [&](auto K) { if constexpr (on) exph(IC<j>{}, IC<e0 + K.v>{}); else rsc(K); };
        if constexpr (SG.v < 0) sfor<0, ne>([&](auto K) { eb(K); __builtin_amdgcn_sched_barrier(0); });
        else seg(IC<(SG.v & 1)>{}, NRD, rd, IC<ne>{}, eb, NV, ex);
      };
      sfor<0, NR>([&](auto R) { rd1(IC<0>{}, IC<0>{}, R); });
      __builtin_amdgcn_sched_barrier(0);
      if constexpr (HALF == 0) pvseg(IC<-1>{}, IC<0>{}, nop, IC<0>{}, nop);
      sfor<0, 8>([&](auto S) {
        constexpr int i = S.v;
        constexpr int pv = i == 0 ? 0 : i == 5 ? 2 : 3, p1 = (pv + 1) / 2;
        pvseg(IC<2 * i>{}, IC<NR>{}, [&](auto R) { rd1(S, IC<1>{}, R); }, IC<pv - p1 + (i == 7 ? NQ : 0)>{},
              [&](auto K) { if constexpr (K.v < pv - p1) vm(IC<i - 1>{}, IC<p1 + K.v>{}); else rsr(IC<K.v - (pv - p1)>{}); });
        if constexpr (i <= 4) wait_vm<6>();
        else if constexpr (i == 5) { if (u == 0) wait_vm<9>(); else wait_vm<5>(); }
        else if constexpr (i == 6) { if (u == 0) wait_vm<15>(); else wait_vm<2>(); }
        else { if (u == 0) wait_vm<9>(); }
        bar();
        if constexpr (i == 4) { mkK(); mkWV(offW, HD * 2); }
        if constexpr (i == 5) mkX();
        constexpr int cv = i == 4 ? 2 : 3;
        pvseg(IC<2 * i + 1>{}, IC<(i < 7 ? NR : 0)>{}, [&](auto R) { rd1(IC<i + 1>{}, IC<0>{}, R); }, IC<(i < 7 ? (cv + 1) / 2 : cv)>{}, [&](auto K) { vm(S, K); });
      });
      sfor<0, 6>([&](auto S) { undef8(kf[2][S.v]); });
      char* ob = p.ofr + ((size_t)((br * 8 + b) * 192 + (h0 + 4 * u) * 6) * 9 + QT0) * 1024 + (lid() << 4);
      const int sf = ut[0] >> 1, sh = ut[2] >> 1, hp = ut[2] & 1;
      sfor<0, NQ>([&](auto Q) {
        float r = rs[Q.v];
        f32x4 a0 = acc[0][Q.v] * r, a1 = acc[1][Q.v] * r, a2 = acc[2][Q.v] * r;
        ost(ob + (sf * 9 + Q.v) * 1024, u32x4{pk2(a0[0], a0[1]), pk2(a0[2], a0[3]), pk2(a1[0], a1[1]), pk2(a1[2], a1[3])});
        ost(ob + (sh * 9 + Q.v) * 1024 + hp * 8, u32x2{pk2(a2[0], a2[1]), pk2(a2[2], a2[3])});
      });
      __builtin_amdgcn_sched_barrier(0);
    }
  }
  wait_vm<0>();
}

__global__ __launch_bounds__(512, 2) void k1_attn(P1 p) {
  extern __shared__ __attribute__((aligned(16))) char smem[];
  const int w = __builtin_amdgcn_readfirstlane(threadIdx.x >> 6), lane = threadIdx.x & 63;
  const int xcd = blockIdx.x & 7, idx = blockIdx.x >> 3;
  const int br = xcd >> 2, h0 = 8 * (xcd & 3) + (idx & 3), b = idx >> 2;
  if (threadIdx.x < 8) p.cnt[(blockIdx.x * 2 + (threadIdx.x >> 2)) * 32 + (threadIdx.x & 3)] = 0u;
  if (w < 4) k1_body<5, 0, 0>(p, w, lane, br, h0, b);
  else k1_body<4, 5, 1>(p, w, lane, br, h0, b);
}

constexpr int OBUF = 38912, OWO = 18432, LDS2 = 4 * OBUF + 1024;
struct P2 {
  const char* ofr; const char* wo[2]; char* slab; unsigned* cnt; char* out;
};
template <int NQ, int QT0, int NN>
__device__ __forceinline__ void k2_body(const P2& p, int w, int lane, int n0, int br, int b, int nt, int ksp) {
  const int g = lane >> 4, c = lane & 15;
  unsigned offW[3];
  sfor<0, 3>([&](auto I) {
    int o = 1024 * I.v + 16 * lane, rr = o / 320, ch = (o % 320) >> 4;
    offW[I.v] = (8 * w + rr) * 1280 + (((((ch >> 1) ^ ((rr >> 2) & 1)) << 1) | (ch & 1)) << 4);
  });
  const unsigned offO = 2304 * w + 16 * lane;
  const char* ob = p.ofr + (size_t)((br * 8 + b) * 192 + ksp * 48) * 9216;
  const char* wb = (br ? p.wo[1] : p.wo[0]) + (size_t)ksp * 1536 * 1280 + nt * 320;
  auto vm = [&](int i, auto BUF, auto N) {
    constexpr int n = N.v;
    if constexpr (n < 3) {
      unsigned l = BUF.v * OBUF + 2304 * w + n * 1024;
      if constexpr (n < 2) dma16(offO, ob + i * OWO + n * 1024, l);
      else if (lane < 16) dma16(offO, ob + i * OWO + n * 1024, l);
    } else {
      unsigned l = BUF.v * OBUF + OWO + 2560 * w + (n - 3) * 1024;
      if constexpr (n < 5) dma16(offW[n - 3], wb + i * 64 * 1280, l);
      else if (lane < 32) dma16(offW[2], wb + i * 64 * 1280, l);
    }
  };
  const unsigned rowp = OWO + (4 * g + (c >> 2)) * 320 + (c & 3) * 8 + n0 * 32;
  const unsigned aT0 = rowp + ((n0 & 1) ? -(g & 1) * 32 : (g & 1) * 32), aT1 = rowp + ((n0 & 1) ? (g & 1) * 32 : -(g & 1) * 32);
  const unsigned aO = QT0 * 1024 + lane * 16;
  constexpr int NM = NN * NQ, NR = NN + NQ;
  bf16x8 fa[2][NN], fb[2][NQ];
  f32x4 acc[NM];
  sfor<0, NM>([&](auto M) { acc[M.v] = f32x4{0, 0, 0, 0}; });
  lp pO, pT0, pT1;
  auto setp = [&](int bufi) { pO = mkp(aO + bufi * OBUF); pT0 = mkp(aT0 + bufi * OBUF); pT1 = mkp(aT1 + bufi * OBUF); };
  auto rd1 = [&](auto KS, auto R) {
    constexpr int ks = KS.v, r = R.v;
    if constexpr (r == 0) fb[ks][0] = lds128(pO + ks * 9216);
    else if constexpr (r <= NN) fa[ks][r - 1] = ldstr((((r - 1) & 1) ? pT1 : pT0) + (r - 1) * 32 + ks * 10240, 5120);
    else fb[ks][r - NN] = lds128(pO + ks * 9216 + (r - NN) * 1024);
  };
  auto seg = [&](auto KS, auto NI, auto&& item) {
    sfor<0, NM>([&](auto M) {
      acc[M.v] = mma(fa[KS.v][M.v % NN], fb[KS.v][M.v / NN], acc[M.v]);
      __builtin_amdgcn_sched_barrier(0);
      constexpr int per = (NI.v + NM - 1) / NM, lo = M.v * per < NI.v ? M.v * per : NI.v, hi = lo + per < NI.v ? lo + per : NI.v;
      sfor<lo, hi>([&](auto K) { item(K); __builtin_amdgcn_sched_barrier(0); });
    });
  };
  const int grp = (br * 8 + b) * 4 + nt;
  sfor<0, 4>([&](auto S) { sfor<0, 6>([&](auto N) { vm(S.v, S, N); }); });
  if (w == 0) dma4((lane & 31) * 8192 + ((lane >> 3) & 3) * 2048, p.slab + (size_t)grp * 262144, 4 * OBUF);
  else if (w == 1) dma4(((lane * 9) >> 2) * 2560, p.out + (size_t)b * 144 * 2560, 4 * OBUF + 256);
  wait_vm<18>(); bar();
  setp(0);
  sfor<0, NR>([&](auto R) { rd1(IC<0>{}, R); });
  __builtin_amdgcn_sched_barrier(0);
  auto stage = [&](int i, auto J, auto LAST) {
    constexpr int j = J.v;
    constexpr int pw = (LAST.v && j > 0) ? 0 : 3;
    seg(IC<0>{}, IC<NR + pw>{}, [&](auto K) {
      if constexpr (K.v < NR) rd1(IC<1>{}, K);
      else if (i > 0) vm(i + 3, IC<(j + 3) & 3>{}, IC<3 + K.v - NR>{});
    });
    if constexpr (!LAST.v || j == 0) wait_vm<12>(); else if constexpr (j == 1) wait_vm<6>(); else if constexpr (j == 2) wait_vm<0>();
    bar();
    constexpr bool fin = LAST.v && j == 3;
    if constexpr (!fin) setp((j + 1) & 3);
    seg(IC<1>{}, IC<(fin ? 0 : NR) + (LAST.v ? 0 : 3)>{}, [&](auto K) {
      if constexpr (K.v < NR) rd1(IC<0>{}, K); else vm(i + 4, J, IC<K.v - NR>{});
    });
  };
#pragma unroll 1
  for (int it = 0; it < 5; ++it) sfor<0, 4>([&](auto J) { stage(4 * it + J.v, J, IC<0>{}); });
  sfor<0, 4>([&](auto J) { stage(20 + J.v, J, IC<1>{}); });
  char* sl = p.slab + (size_t)(grp * 32 + w) * 8192 + lane * 16;
  unsigned* fl = p.cnt + (grp * 8 + w) * 32;
  auto tail = [&](auto KSP) {
    constexpr int ks = KSP.v;
    float mf = 0.f;
    sfor<0, NM>([&](auto M) { if constexpr ((M.v & 3) != ks) {
      mf = fmaxf(mf, fmaxf(fmaxf(fabsf(acc[M.v][0]), fabsf(acc[M.v][1])), fmaxf(fabsf(acc[M.v][2]), fabsf(acc[M.v][3])))); } });
    unsigned e = (wave_umax(__float_as_uint(mf)) >> 23) + 1;
    e = e > 253u ? 253u : e;
    const float inv = __uint_as_float((254u - e) << 23);
    sfor<0, 8>([&](auto I) {
      constexpr int d = I.v >> 1, m0 = d + 8 * (I.v & 1), m1 = m0 + 4;
      if constexpr (d != ks && m0 < NM) {
        f32x4 x = acc[m0] * inv;
        char* a = sl + (ks * 32 + d) * 2048 + (I.v & 1) * 1024;
        if constexpr (m1 < NM) { f32x4 y = acc[m1] * inv; st_sc1(a, u32x4{pkn(x[0], x[1]), pkn(x[2], x[3]), pkn(y[0], y[1]), pkn(y[2], y[3])}); }
        else st2_sc1(a, u32x2{pkn(x[0], x[1]), pkn(x[2], x[3])});
      }
    });
    wait_vm<0>();
    if (lane == 0) __hip_atomic_store(fl + ks, e, __ATOMIC_RELAXED, __HIP_MEMORY_SCOPE_AGENT);
    unsigned f;
    do { __builtin_amdgcn_s_sleep(2); f = __hip_atomic_load(fl + (lane & 3), __ATOMIC_RELAXED, __HIP_MEMORY_SCOPE_AGENT); } while (__builtin_amdgcn_ballot_w64(f == 0u) != 0ull);
    u32x4 r4[2][3]; u32x2 r2[3];
    sfor<0, 6>([&](auto I) {
      constexpr int o = I.v >> 1, h = I.v & 1, src = o < ks ? o : o + 1, m0 = ks + 8 * h, m1 = m0 + 4;
      const char* a = sl + (src * 32 + ks) * 2048 + h * 1024;
      if constexpr (m1 < NM) ld_sc1(r4[h][o], a); else if constexpr (m0 < NM) ld2_sc1(r2[o], a);
    });
    float sc[3];
    sfor<0, 3>([&](auto O) { constexpr int src = O.v < ks ? O.v : O.v + 1;
      sc[O.v] = __uint_as_float(__builtin_amdgcn_readlane(f, src) << 23) * (1.f / 32767.f); });
    __builtin_amdgcn_sched_barrier(0);
    wait_vm<0>();
    sfor<0, 6>([&](auto I) {
      constexpr int o = I.v >> 1, h = I.v & 1, m0 = ks + 8 * h, m1 = m0 + 4;
      if constexpr (m1 < NM) pinu4(r4[h][o]); else if constexpr (m0 < NM) pinu2(r2[o]);
    });
    sfor<0, 4>([&](auto K) {
      constexpr int m = ks + 4 * K.v, h = K.v >> 1;
      if constexpr (m < NM) {
        f32x4 v[4];
        sfor<0, 4>([&](auto S) {
          if constexpr (S.v == ks) v[S.v] = acc[m];
          else {
            constexpr int o = S.v < ks ? S.v : S.v - 1;
            int a0, a1;
            if constexpr (ks + 8 * h + 4 < NM) { a0 = r4[h][o][2 * (K.v & 1)]; a1 = r4[h][o][2 * (K.v & 1) + 1]; } else { a0 = r2[o][0]; a1 = r2[o][1]; }
            v[S.v] = f32x4{(float)(short)a0, (float)(a0 >> 16), (float)(short)a1, (float)(a1 >> 16)} * sc[o];
          }
        });
        f32x4 o = (v[0] + v[1]) + (v[2] + v[3]);
        *(u32x2*)(p.out + ((size_t)(b * 144 + 16 * (QT0 + m / NN) + c) * 1280 + br * 640 + nt * 160 + 16 * (n0 + m % NN) + 4 * g) * 2) =
            u32x2{pk2(o[0], o[1]), pk2(o[2], o[3])};
      }
    });
  };
  if (ksp == 0) tail(IC<0>{}); else if (ksp == 1) tail(IC<1>{}); else if (ksp == 2) tail(IC<2>{}); else tail(IC<3>{});
}
__global__ __launch_bounds__(512, 2) void k2_oproj(P2 p) {
  extern __shared__ __attribute__((aligned(16))) char smem[];
  const int w = __builtin_amdgcn_readfirstlane(threadIdx.x >> 6), lane = threadIdx.x & 63;
  const int xcd = blockIdx.x & 7, idx = blockIdx.x >> 3;
  const int br = xcd >> 2, b = idx >> 2, nt = idx & 3, ksp = xcd & 3;
  if (w < 2) k2_body<5, 0, 3>(p, w, lane, 3 * w, br, b, nt, ksp);
  else if (w < 4) k2_body<5, 0, 2>(p, w, lane, 2 * w + 2, br, b, nt, ksp);
  else if (w < 6) k2_body<4, 5, 2>(p, w, lane, 2 * w - 8, br, b, nt, ksp);
  else k2_body<4, 5, 3>(p, w, lane, 3 * w - 14, br, b, nt, ksp);
}

extern "C" void kernel_launch(void* const* d_in, const int* in_sizes, int n_in, void* d_out, int out_size, void* d_ws,
                              size_t ws_size, hipStream_t stream) {
  P1 p;
  p.x[0] = (const char*)d_in[0]; p.k[0] = (const char*)d_in[1]; p.v[0] = (const char*)d_in[2]; p.wq[0] = (const char*)d_in[3];
  p.x[1] = (const char*)d_in[5]; p.k[1] = (const char*)d_in[6]; p.v[1] = (const char*)d_in[7]; p.wq[1] = (const char*)d_in[8];
  p.wo[0] = (const char*)d_in[4]; p.wo[1] = (const char*)d_in[9];
  p.ofr = (char*)d_ws;
  p.cnt = (unsigned*)((char*)d_ws + (30u << 20));
  P2 q;
  q.ofr = (const char*)d_ws; q.wo[0] = (const char*)d_in[4]; q.wo[1] = (const char*)d_in[9];
  q.slab = (char*)d_ws + (32u << 20); q.cnt = p.cnt; q.out = (char*)d_out;
  (void)hipFuncSetAttribute((const void*)k1_attn, hipFuncAttributeMaxDynamicSharedMemorySize, LDS1);
  (void)hipFuncSetAttribute((const void*)k2_oproj, hipFuncAttributeMaxDynamicSharedMemorySize, LDS2);
  k1_attn<<<256, 512, LDS1, stream>>>(p);
  k2_oproj<<<256, 512, LDS2, stream>>>(q);
}
```
